```python
import torch

def _softmax(x, axis=-1):
    return torch.softmax(x, dim=axis)
def _gelu(x):
    return torch.nn.functional.gelu(x, approximate="tanh")
def _silu(x):
    return torch.nn.functional.silu(x)
def _sigmoid(x):
    return torch.sigmoid(x)
def _layernorm(x, axis=-1, eps=1e-5):
    m = x.mean(dim=axis, keepdim=True)
    v = x.var(dim=axis, keepdim=True, unbiased=False)
    return (x - m) / torch.sqrt(v + eps)
def _scatter_add(tgt, idx, src, axis=0):
    return tgt.clone().index_add_(axis, idx, src)
def _segment_sum(src, seg, n):
    out = torch.zeros((n,) + src.shape[1:], dtype=src.dtype)
    return out.index_add_(0, seg, src)
def _pad(x, pw):
    flat = tuple(v for pair in reversed(pw) for v in pair)
    return torch.nn.functional.pad(x, flat)

kernel_name = 'standalone__w4a16_gemm_dequant__prompt'

def setup_inputs():

    _w = (lambda a: (lambda t: t.to(torch.bfloat16) if t.is_floating_point() else t)(torch.as_tensor(a)))
    B, S, D, F = 1, 2048, 1024, 3584
    G = 128 if 128 > 0 else D
    D = (D // G) * G
    ng = D // G
    qw = torch.randint(0, 16, (D, F)).to(torch.int32)
    return {
        "x": _w((lambda *s: torch.randn(*s, dtype=torch.bfloat16) * 0.05)(B, S, D)),
        "qw": _w(qw),
        "scales": _w(torch.abs((lambda *s: torch.randn(*s, dtype=torch.bfloat16) * 0.05)(ng, F)) + 0.01),
        "zeros": _w(torch.full((ng, F), 8.0, dtype=float)),
    }

def reference(x, qw, scales, zeros):
    B, S, D = x.shape
    ng, F = scales.shape
    G = D // ng
    s = (lambda x, n, axis: torch.repeat_interleave(x, n, dim=axis))(scales, G, axis=0)
    z = (lambda x, n, axis: torch.repeat_interleave(x, n, dim=axis))(zeros, G, axis=0)
    w = ((lambda x, dt: x.to(dt))(qw, x.dtype) - z) * s
    return x @ w
```

```cpp
#include <hip/hip_runtime.h>
#include <hip/hip_bf16.h>
#include <stdint.h>

using bf16x8 = __attribute__((ext_vector_type(8))) short;
using f32x4 = __attribute__((ext_vector_type(4))) float;
typedef unsigned short u16;

__device__ __forceinline__ float bf2f(u16 v) { return __uint_as_float(((unsigned)v) << 16); }
__device__ __forceinline__ u16 f2bf(float f) {
    __hip_bfloat16 b = __float2bfloat16(f);
    return *reinterpret_cast<u16*>(&b);
}

constexpr int M_ = 2048, K_ = 1024, N_ = 3584, G_ = 128;

__global__ __launch_bounds__(256) void dequant_k(const int* __restrict__ qw, const u16* __restrict__ sc,
                                                 const u16* __restrict__ zr, u16* __restrict__ wt) {
    int n = blockIdx.x * 64 + (threadIdx.x & 63);
    int k0 = blockIdx.y * 32 + (threadIdx.x >> 6) * 8;
    int g = k0 / G_;
    float s = bf2f(sc[g * N_ + n]), z = bf2f(zr[g * N_ + n]);
    bf16x8 o;
#pragma unroll
    for (int j = 0; j < 8; ++j) {
        int q = qw[(k0 + j) * N_ + n];
        float t = bf2f(f2bf((float)q));
        t = bf2f(f2bf(t - z));
        o[j] = (short)f2bf(t * s);
    }
    *reinterpret_cast<bf16x8*>(wt + (size_t)n * K_ + k0) = o;
}

constexpr int BM = 128, BK = 32, HT = BM * BK;
__global__ __launch_bounds__(256, 2) void gemm_k(const u16* A, const u16* Bt, u16* C) {
    __shared__ __attribute__((aligned(16))) u16 shm[2 * HT];
    const int M = M_, N = N_, K = K_;
    int blk = blockIdx.x;
    int brow = (blk / (N / BM)) * BM, bcol = (blk % (N / BM)) * BM;
    int wid = threadIdx.x >> 6, lane = threadIdx.x & 63, wr = wid >> 1, wc = wid & 1, fr = lane & 15, fq = lane >> 4;
    u16 *SA = shm, *SB = shm + HT;
    auto stage = [&](int kt) {
        for (int i = 0; i < 2; ++i) {
            int b = threadIdx.x * 16 + i * 4096, r = b / 64, c = (b % 64) / 2;
            __builtin_amdgcn_global_load_lds((const unsigned*)(A + (long)(brow + r) * K + kt * BK + c), (__attribute__((address_space(3))) unsigned*)((char*)SA + b), 16, 0, 0);
            __builtin_amdgcn_global_load_lds((const unsigned*)(Bt + (long)(bcol + r) * K + kt * BK + c), (__attribute__((address_space(3))) unsigned*)((char*)SB + b), 16, 0, 0);
        }
    };
    auto ld = [&](bf16x8 d[4], u16* base, int off) {
        for (int m = 0; m < 4; ++m) d[m] = *reinterpret_cast<const bf16x8*>((char*)base + (off + m * 16 + fr) * BK * 2 + fq * 16);
    };
    f32x4 acc[4][4] = {};
    bf16x8 At[4], Btl[4];
    for (int t = 0; t < K / BK; ++t) {
        stage(t);
        asm volatile("s_waitcnt vmcnt(0)" ::: "memory");
        __syncthreads();
        ld(At, SA, wr * 64);
        ld(Btl, SB, wc * 64);
        for (int m = 0; m < 4; ++m)
            for (int n = 0; n < 4; ++n) acc[m][n] = __builtin_amdgcn_mfma_f32_16x16x32_bf16(At[m], Btl[n], acc[m][n], 0, 0, 0);
        __syncthreads();
    }
    for (int m = 0; m < 4; ++m)
        for (int n = 0; n < 4; ++n)
            for (int j = 0; j < 4; ++j)
                C[(long)(brow + wr * 64 + m * 16 + fq * 4 + j) * N + (bcol + wc * 64 + n * 16 + fr)] = f2bf(acc[m][n][j]);
}

extern "C" void kernel_launch(void* const* d_in, const int* in_sizes, int n_in, void* d_out, int out_size, void* d_ws,
                              size_t ws_size, hipStream_t stream) {
    const u16* x = (const u16*)d_in[0];
    const int* qw = (const int*)d_in[1];
    const u16* sc = (const u16*)d_in[2];
    const u16* zr = (const u16*)d_in[3];
    u16* wt = (u16*)d_ws;
    dequant_k<<<dim3(N_ / 64, K_ / 32), 256, 0, stream>>>(qw, sc, zr, wt);
    gemm_k<<<(M_ / BM) * (N_ / BM), 256, 0, stream>>>(x, wt, (u16*)d_out);
}
```

```cpp
#include <hip/hip_runtime.h>
#include <stdint.h>
typedef unsigned short u16;
constexpr unsigned LDS_BYTES = 147968;

__global__ __launch_bounds__(512, 2) void w4a16_fused(const u16* __restrict__ x, const int* __restrict__ qw,
                                                      const u16* __restrict__ sc, const u16* __restrict__ zr,
                                                      u16* __restrict__ out) {
    __shared__ __attribute__((aligned(1024))) char lds[LDS_BYTES];
    asm volatile(
      "Lstart_%=:\n\t"
      "s_load_dwordx8 s[0:7], %0, 0x0\n\t"
      "s_load_dwordx2 s[32:33], %0, 0x20\n\t"
      "s_mov_b32 s28, %1\n\t"
      "v_readfirstlane_b32 s24, %2\n\t"
      "v_and_b32 v1, 63, %2\n\t"
      "s_and_b32 s25, s28, 7\n\t"
      "s_lshr_b32 s26, s28, 3\n\t"
      "s_lshr_b32 s24, s24, 6\n\t"
      "s_cmp_ge_u32 s28, 224\n\t"
      "s_cbranch_scc1 Lhelper_%=\n\t"
      "s_mul_i32 s27, s25, 28\n\t"
      "s_add_u32 s27, s27, s26\n\t"
      "s_and_b32 s25, s27, 7\n\t"
      "s_lshr_b32 s26, s27, 3\n\t"
      "s_cmp_ge_u32 s24, 4\n\t"
      "s_cbranch_scc1 Lcons_%=\n\t"
      "s_lshl_b32 s27, s26, 9\n\t"
      "s_sub_u32 s14, 14680064, s27\n\t"
      "s_mov_b32 s15, 0x20000\n\t"
      "v_lshrrev_b32 v9, 5, v1\n\t"
      "v_and_b32 v10, 31, v1\n\t"
      "v_mul_u32_u24 v3, 114688, v9\n\t"
      "s_mul_i32 s42, s24, 229376\n\t"
      "v_lshl_add_u32 v3, v10, 4, v3\n\t"
      "v_mov_b32 v128, v3\n\t"
      "v_add_u32 v129, 14336, v3\n\t"
      "v_add_u32 v130, 28672, v3\n\t"
      "v_add_u32 v131, 43008, v3\n\t"
      "v_add_u32 v132, 57344, v3\n\t"
      "v_add_u32 v133, 71680, v3\n\t"
      "v_add_u32 v134, 86016, v3\n\t"
      "v_add_u32 v135, 100352, v3\n\t"
      "s_waitcnt lgkmcnt(0)\n\t"
      "s_add_u32 s12, s2, s27\n\t"
      "s_addc_u32 s13, s3, 0\n\t"
      "s_and_b32 s13, s13, 0xffff\n\t"
      "buffer_load_dwordx4 v[32:35], v128, s[12:15], s42 offen \n\t"
      "s_lshl_b32 s27, s25, 2\n\t"
      "s_add_u32 s27, s27, s24\n\t"
      "s_lshl_b32 s27, s27, 17\n\t"
      "buffer_load_dwordx4 v[36:39], v129, s[12:15], s42 offen \n\t"
      "s_add_u32 s8, s0, s27\n\t"
      "s_addc_u32 s9, s1, 0\n\t"
      "s_and_b32 s9, s9, 0xffff\n\t"
      "buffer_load_dwordx4 v[40:43], v130, s[12:15], s42 offen \n\t"
      "s_mov_b32 s10, 0x20000\n\t"
      "s_mov_b32 s11, 0x20000\n\t"
      "s_lshl_b32 s16, s24, 13\n\t"
      "buffer_load_dwordx4 v[44:47], v131, s[12:15], s42 offen \n\t"
      "v_lshrrev_b32 v11, 3, v1\n\t"
      "v_and_b32 v12, 6, v11\n\t"
      "v_xor_b32 v12, v12, v1\n\t"
      "buffer_load_dwordx4 v[48:51], v132, s[12:15], s42 offen \n\t"
      "v_and_b32 v12, 7, v12\n\t"
      "v_lshlrev_b32 v11, 11, v11\n\t"
      "v_lshl_add_u32 v2, v12, 4, v11\n\t"
      "buffer_load_dwordx4 v[52:55], v133, s[12:15], s42 offen \n\t"
      "s_mov_b32 s41, 0\n\t"
      "s_mov_b32 m0, s16\n\t"
      "buffer_load_dwordx4 v[56:59], v134, s[12:15], s42 offen \n\t"
      "buffer_load_dwordx4 v[60:63], v135, s[12:15], s42 offen \n\t"
      "v_mov_b32 v120, v2\n\t"
      "buffer_load_dwordx4 v120, s[8:11], s41 offen offset:0  lds\n\t"
      "s_mov_b32 s20, s0\n\t"
      "s_and_b32 s21, s1, 0xffff\n\t"
      "s_mov_b32 s22, 4194304\n\t"
      "s_mov_b32 s23, 0x20000\n\t"
      "v_add_u32 v121, 15360, v2\n\t"
      "buffer_load_dwordx4 v121, s[8:11], s41 offen offset:1024  lds\n\t"
      "s_lshl_b32 s27, s28, 2\n\t"
      "s_add_u32 s27, s27, s24\n\t"
      "s_mul_i32 s27, s27, 19\n\t"
      "v_add_u32 v122, 30720, v2\n\t"
      "buffer_load_dwordx4 v122, s[8:11], s41 offen offset:2048  lds\n\t"
      "v_add_u32 v8, s27, v1\n\t"
      "v_and_b32 v11, 7, v8\n\t"
      "v_lshrrev_b32 v8, 3, v8\n\t"
      "v_lshlrev_b32 v8, 11, v8\n\t"
      "v_lshl_add_u32 v8, v11, 7, v8\n\t"
      "v_cmp_gt_u32 vcc, 19, v1\n\t"
      "v_mov_b32 v11, 0x40000000\n\t"
      "v_add_u32 v123, 46080, v2\n\t"
      "buffer_load_dwordx4 v123, s[8:11], s41 offen offset:3072  lds\n\t"
      "v_cndmask_b32 v8, v11, v8, vcc\n\t"
      "v_bfe_u32 v11, v10, 1, 2\n\t"
      "v_lshlrev_b32 v11, 2, v11\n\t"
      "v_add_u32 v124, 65536, v2\n\t"
      "s_add_u32 m0, s16, 4096\n\t"
      "s_nop 0\n\t"
      "buffer_load_dwordx4 v124, s[8:11], s41 offen offset:0  lds\n\t"
      "v_bfe_u32 v12, v10, 3, 1\n\t"
      "v_bfe_u32 v13, v10, 2, 1\n\t"
      "v_xor_b32 v12, v12, v13\n\t"
      "v_add_u32 v125, 80896, v2\n\t"
      "buffer_load_dwordx4 v125, s[8:11], s41 offen offset:1024  lds\n\t"
      "v_lshl_or_b32 v11, v12, 1, v11\n\t"
      "v_and_or_b32 v11, v10, 1, v11\n\t"
      "v_lshl_or_b32 v11, v9, 4, v11\n\t"
      "v_add_u32 v126, 96256, v2\n\t"
      "buffer_load_dwordx4 v126, s[8:11], s41 offen offset:2048  lds\n\t"
      "s_and_b32 s27, s24, 1\n\t"
      "s_lshl_b32 s27, s27, 9\n\t"
      "s_lshr_b32 s29, s24, 1\n\t"
      "s_mul_i32 s29, s29, 8192\n\t"
      "s_add_u32 s27, s27, s29\n\t"
      "s_add_u32 s27, s27, 98304\n\t"
      "v_add_u32 v127, 111616, v2\n\t"
      "buffer_load_dwordx4 v127, s[8:11], s41 offen offset:3072  lds\n\t"
      "v_lshl_add_u32 v4, v11, 4, s27\n\t"
      "v_lshrrev_b32 v12, 4, v10\n\t"
      "v_lshl_add_u32 v4, v12, 12, v4\n\t"
      "buffer_load_dword v145, v8, s[20:23], 0 offen\n\t"
      "s_mov_b32 s46, 0\n\t"
      "s_mov_b32 s40, 0x4b800000\n\t"
      "s_mov_b32 s39, 0\n\t"
      "s_mov_b32 s44, 0\n\t"
      "s_mov_b32 s47, 0\n\t"
      "v_mov_b32 v148, 1\n\t"
      "v_lshlrev_b32 v149, 4, v10\n\t"
      "v_add_u32 v149, 131072, v149\n\t"
      "v_mov_b32 v6, 147456\n\t"
      "v_lshl_add_u32 v5, v1, 2, v6\n\t"
      "v_add_u32 v5, 64, v5\n\t"
      "v_cmp_eq_u32 vcc, 0, v1\n\t"
      "s_nop 1\n\t"
      "v_cndmask_b32 v5, v5, v6, vcc\n\t"
      "s_barrier\n\t"
      "s_cmp_lt_u32 s24, 1\n\t"
      "s_cbranch_scc1 Lnd_%=\n\t"
      "s_sleep 3\n\t"
      "s_cmp_lt_u32 s24, 2\n\t"
      "s_cbranch_scc1 Lnd_%=\n\t"
      "s_sleep 3\n\t"
      "s_cmp_lt_u32 s24, 3\n\t"
      "s_cbranch_scc1 Lnd_%=\n\t"
      "s_sleep 3\n\t"
      "Lnd_%=:\n\t"
      "ds_read_b128 v[112:115], v6 offset:16\n\t"
      "s_waitcnt lgkmcnt(0)\n\t"
      "v_and_b32 v112, v112, v113\n\t"
      "v_and_b32 v114, v114, v115\n\t"
      "v_and_b32 v112, v112, v114\n\t"
      "s_nop 0\n\t"
      "v_readfirstlane_b32 s48, v112\n\t"
      "s_nop 1\n\t"
      "s_cmp_eq_u32 s48, 1\n\t"
      "s_cbranch_scc0 Lp_slow_%=\n\t"
      "ds_read_b128 v[136:139], v149 offset:1024\n\t"
      "ds_read_b128 v[140:143], v149 offset:1536\n\t"
      "v_add_u32 v149, 2048, v149\n\t"
      "Lp_loopf_%=:\n\t"
      "s_add_u32 s42, s42, 917504\n\t"
      "buffer_load_dwordx4 v[64:67], v128, s[12:15], s42 offen \n\t"
      "s_waitcnt vmcnt(13) lgkmcnt(0)\n\t"
      "v_fma_mix_f32 v112, v32, v136, v140 op_sel_hi:[1,0,0]\n\t"
      "v_fma_mix_f32 v113, v33, v137, v141 op_sel_hi:[1,0,0]\n\t"
      "v_fma_mix_f32 v114, v34, v138, v142 op_sel_hi:[1,0,0]\n\t"
      "v_fma_mix_f32 v115, v35, v139, v143 op_sel_hi:[1,0,0]\n\t"
      "buffer_load_dwordx4 v[68:71], v129, s[12:15], s42 offen \n\t"
      "v_fma_mix_f32 v116, v36, v136, v140 op_sel_hi:[1,0,0]\n\t"
      "v_fma_mix_f32 v117, v37, v137, v141 op_sel_hi:[1,0,0]\n\t"
      "v_fma_mix_f32 v118, v38, v138, v142 op_sel_hi:[1,0,0]\n\t"
      "v_fma_mix_f32 v119, v39, v139, v143 op_sel_hi:[1,0,0]\n\t"
      "buffer_load_dwordx4 v[72:75], v130, s[12:15], s42 offen \n\t"
      "v_cvt_pk_bf16_f32 v96, v112, v116\n\t"
      "v_cvt_pk_bf16_f32 v100, v113, v117\n\t"
      "v_cvt_pk_bf16_f32 v104, v114, v118\n\t"
      "v_cvt_pk_bf16_f32 v108, v115, v119\n\t"
      "v_fma_mix_f32 v112, v40, v136, v140 op_sel_hi:[1,0,0]\n\t"
      "v_fma_mix_f32 v113, v41, v137, v141 op_sel_hi:[1,0,0]\n\t"
      "v_fma_mix_f32 v114, v42, v138, v142 op_sel_hi:[1,0,0]\n\t"
      "v_fma_mix_f32 v115, v43, v139, v143 op_sel_hi:[1,0,0]\n\t"
      "buffer_load_dwordx4 v[76:79], v131, s[12:15], s42 offen \n\t"
      "v_fma_mix_f32 v116, v44, v136, v140 op_sel_hi:[1,0,0]\n\t"
      "v_fma_mix_f32 v117, v45, v137, v141 op_sel_hi:[1,0,0]\n\t"
      "v_fma_mix_f32 v118, v46, v138, v142 op_sel_hi:[1,0,0]\n\t"
      "v_fma_mix_f32 v119, v47, v139, v143 op_sel_hi:[1,0,0]\n\t"
      "buffer_load_dwordx4 v[80:83], v132, s[12:15], s42 offen \n\t"
      "v_cvt_pk_bf16_f32 v97, v112, v116\n\t"
      "v_cvt_pk_bf16_f32 v101, v113, v117\n\t"
      "v_cvt_pk_bf16_f32 v105, v114, v118\n\t"
      "v_cvt_pk_bf16_f32 v109, v115, v119\n\t"
      "s_waitcnt vmcnt(13)\n\t"
      "v_fma_mix_f32 v112, v48, v136, v140 op_sel_hi:[1,0,0]\n\t"
      "v_fma_mix_f32 v113, v49, v137, v141 op_sel_hi:[1,0,0]\n\t"
      "v_fma_mix_f32 v114, v50, v138, v142 op_sel_hi:[1,0,0]\n\t"
      "v_fma_mix_f32 v115, v51, v139, v143 op_sel_hi:[1,0,0]\n\t"
      "buffer_load_dwordx4 v[84:87], v133, s[12:15], s42 offen \n\t"
      "v_fma_mix_f32 v116, v52, v136, v140 op_sel_hi:[1,0,0]\n\t"
      "v_fma_mix_f32 v117, v53, v137, v141 op_sel_hi:[1,0,0]\n\t"
      "v_fma_mix_f32 v118, v54, v138, v142 op_sel_hi:[1,0,0]\n\t"
      "v_fma_mix_f32 v119, v55, v139, v143 op_sel_hi:[1,0,0]\n\t"
      "buffer_load_dwordx4 v[88:91], v134, s[12:15], s42 offen \n\t"
      "v_cvt_pk_bf16_f32 v98, v112, v116\n\t"
      "v_cvt_pk_bf16_f32 v102, v113, v117\n\t"
      "v_cvt_pk_bf16_f32 v106, v114, v118\n\t"
      "v_cvt_pk_bf16_f32 v110, v115, v119\n\t"
      "ds_read_b32 v144, v6 offset:8\n\t"
      "v_fma_mix_f32 v112, v56, v136, v140 op_sel_hi:[1,0,0]\n\t"
      "v_fma_mix_f32 v113, v57, v137, v141 op_sel_hi:[1,0,0]\n\t"
      "v_fma_mix_f32 v114, v58, v138, v142 op_sel_hi:[1,0,0]\n\t"
      "v_fma_mix_f32 v115, v59, v139, v143 op_sel_hi:[1,0,0]\n\t"
      "buffer_load_dwordx4 v[92:95], v135, s[12:15], s42 offen \n\t"
      "v_fma_mix_f32 v116, v60, v136, v140 op_sel_hi:[1,0,0]\n\t"
      "v_fma_mix_f32 v117, v61, v137, v141 op_sel_hi:[1,0,0]\n\t"
      "v_fma_mix_f32 v118, v62, v138, v142 op_sel_hi:[1,0,0]\n\t"
      "v_fma_mix_f32 v119, v63, v139, v143 op_sel_hi:[1,0,0]\n\t"
      "v_cvt_pk_bf16_f32 v99, v112, v116\n\t"
      "v_cvt_pk_bf16_f32 v103, v113, v117\n\t"
      "v_cvt_pk_bf16_f32 v107, v114, v118\n\t"
      "v_cvt_pk_bf16_f32 v111, v115, v119\n\t"
      "s_waitcnt lgkmcnt(0)\n\t"
      "Lpc_f0_%=:\n\t"
      "v_readfirstlane_b32 s48, v144\n\t"
      "s_nop 1\n\t"
      "s_cmp_ge_u32 s48, s47\n\t"
      "s_cbranch_scc0 Lpw_f0_%=\n\t"
      "Lpg_f0_%=:\n\t"
      "ds_write_b128 v4, v[96:99] offset:0\n\t"
      "s_add_u32 s46, s46, 32768\n\t"
      "s_cmp_lt_u32 s46, 98304\n\t"
      "ds_write_b128 v4, v[100:103] offset:1024\n\t"
      "s_cselect_b32 s46, s46, 0\n\t"
      "s_add_u32 s41, s41, 128\n\t"
      "ds_write_b128 v4, v[104:107] offset:2048\n\t"
      "s_add_u32 m0, s16, s46\n\t"
      "s_add_u32 s45, m0, 4096\n\t"
      "ds_write_b128 v4, v[108:111] offset:3072\n\t"
      "s_waitcnt vmcnt(8)\n\t"
      "ds_add_u32 v5, v148 offset:0\n\t"
      "buffer_load_dwordx4 v120, s[8:11], s41 offen offset:0  lds\n\t"
      "buffer_load_dwordx4 v121, s[8:11], s41 offen offset:1024  lds\n\t"
      "buffer_load_dwordx4 v122, s[8:11], s41 offen offset:2048  lds\n\t"
      "buffer_load_dwordx4 v123, s[8:11], s41 offen offset:3072  lds\n\t"
      "s_mov_b32 m0, s45\n\t"
      "s_nop 0\n\t"
      "buffer_load_dwordx4 v124, s[8:11], s41 offen offset:0  lds\n\t"
      "buffer_load_dwordx4 v125, s[8:11], s41 offen offset:1024  lds\n\t"
      "buffer_load_dwordx4 v126, s[8:11], s41 offen offset:2048  lds\n\t"
      "buffer_load_dwordx4 v127, s[8:11], s41 offen offset:3072  lds\n\t"
      "s_add_u32 s42, s42, 917504\n\t"
      "buffer_load_dwordx4 v[32:35], v128, s[12:15], s42 offen \n\t"
      "s_waitcnt vmcnt(13) lgkmcnt(0)\n\t"
      "v_fma_mix_f32 v112, v64, v136, v140 op_sel_hi:[1,0,0]\n\t"
      "v_fma_mix_f32 v113, v65, v137, v141 op_sel_hi:[1,0,0]\n\t"
      "v_fma_mix_f32 v114, v66, v138, v142 op_sel_hi:[1,0,0]\n\t"
      "v_fma_mix_f32 v115, v67, v139, v143 op_sel_hi:[1,0,0]\n\t"
      "buffer_load_dwordx4 v[36:39], v129, s[12:15], s42 offen \n\t"
      "v_fma_mix_f32 v116, v68, v136, v140 op_sel_hi:[1,0,0]\n\t"
      "v_fma_mix_f32 v117, v69, v137, v141 op_sel_hi:[1,0,0]\n\t"
      "v_fma_mix_f32 v118, v70, v138, v142 op_sel_hi:[1,0,0]\n\t"
      "v_fma_mix_f32 v119, v71, v139, v143 op_sel_hi:[1,0,0]\n\t"
      "buffer_load_dwordx4 v[40:43], v130, s[12:15], s42 offen \n\t"
      "v_cvt_pk_bf16_f32 v96, v112, v116\n\t"
      "v_cvt_pk_bf16_f32 v100, v113, v117\n\t"
      "v_cvt_pk_bf16_f32 v104, v114, v118\n\t"
      "v_cvt_pk_bf16_f32 v108, v115, v119\n\t"
      "v_fma_mix_f32 v112, v72, v136, v140 op_sel_hi:[1,0,0]\n\t"
      "v_fma_mix_f32 v113, v73, v137, v141 op_sel_hi:[1,0,0]\n\t"
      "v_fma_mix_f32 v114, v74, v138, v142 op_sel_hi:[1,0,0]\n\t"
      "v_fma_mix_f32 v115, v75, v139, v143 op_sel_hi:[1,0,0]\n\t"
      "buffer_load_dwordx4 v[44:47], v131, s[12:15], s42 offen \n\t"
      "v_fma_mix_f32 v116, v76, v136, v140 op_sel_hi:[1,0,0]\n\t"
      "v_fma_mix_f32 v117, v77, v137, v141 op_sel_hi:[1,0,0]\n\t"
      "v_fma_mix_f32 v118, v78, v138, v142 op_sel_hi:[1,0,0]\n\t"
      "v_fma_mix_f32 v119, v79, v139, v143 op_sel_hi:[1,0,0]\n\t"
      "buffer_load_dwordx4 v[48:51], v132, s[12:15], s42 offen \n\t"
      "v_cvt_pk_bf16_f32 v97, v112, v116\n\t"
      "v_cvt_pk_bf16_f32 v101, v113, v117\n\t"
      "v_cvt_pk_bf16_f32 v105, v114, v118\n\t"
      "v_cvt_pk_bf16_f32 v109, v115, v119\n\t"
      "s_waitcnt vmcnt(13)\n\t"
      "v_fma_mix_f32 v112, v80, v136, v140 op_sel_hi:[1,0,0]\n\t"
      "v_fma_mix_f32 v113, v81, v137, v141 op_sel_hi:[1,0,0]\n\t"
      "v_fma_mix_f32 v114, v82, v138, v142 op_sel_hi:[1,0,0]\n\t"
      "v_fma_mix_f32 v115, v83, v139, v143 op_sel_hi:[1,0,0]\n\t"
      "buffer_load_dwordx4 v[52:55], v133, s[12:15], s42 offen \n\t"
      "v_fma_mix_f32 v116, v84, v136, v140 op_sel_hi:[1,0,0]\n\t"
      "v_fma_mix_f32 v117, v85, v137, v141 op_sel_hi:[1,0,0]\n\t"
      "v_fma_mix_f32 v118, v86, v138, v142 op_sel_hi:[1,0,0]\n\t"
      "v_fma_mix_f32 v119, v87, v139, v143 op_sel_hi:[1,0,0]\n\t"
      "buffer_load_dwordx4 v[56:59], v134, s[12:15], s42 offen \n\t"
      "v_cvt_pk_bf16_f32 v98, v112, v116\n\t"
      "v_cvt_pk_bf16_f32 v102, v113, v117\n\t"
      "v_cvt_pk_bf16_f32 v106, v114, v118\n\t"
      "v_cvt_pk_bf16_f32 v110, v115, v119\n\t"
      "ds_read_b32 v144, v6 offset:12\n\t"
      "v_fma_mix_f32 v112, v88, v136, v140 op_sel_hi:[1,0,0]\n\t"
      "v_fma_mix_f32 v113, v89, v137, v141 op_sel_hi:[1,0,0]\n\t"
      "v_fma_mix_f32 v114, v90, v138, v142 op_sel_hi:[1,0,0]\n\t"
      "v_fma_mix_f32 v115, v91, v139, v143 op_sel_hi:[1,0,0]\n\t"
      "buffer_load_dwordx4 v[60:63], v135, s[12:15], s42 offen \n\t"
      "v_fma_mix_f32 v116, v92, v136, v140 op_sel_hi:[1,0,0]\n\t"
      "v_fma_mix_f32 v117, v93, v137, v141 op_sel_hi:[1,0,0]\n\t"
      "v_fma_mix_f32 v118, v94, v138, v142 op_sel_hi:[1,0,0]\n\t"
      "v_fma_mix_f32 v119, v95, v139, v143 op_sel_hi:[1,0,0]\n\t"
      "v_cvt_pk_bf16_f32 v99, v112, v116\n\t"
      "v_cvt_pk_bf16_f32 v103, v113, v117\n\t"
      "v_cvt_pk_bf16_f32 v107, v114, v118\n\t"
      "v_cvt_pk_bf16_f32 v111, v115, v119\n\t"
      "s_waitcnt lgkmcnt(0)\n\t"
      "Lpc_f1_%=:\n\t"
      "v_readfirstlane_b32 s48, v144\n\t"
      "s_nop 1\n\t"
      "s_cmp_ge_u32 s48, s47\n\t"
      "s_cbranch_scc0 Lpw_f1_%=\n\t"
      "Lpg_f1_%=:\n\t"
      "ds_read_b128 v[136:139], v149 offset:1024\n\t"
      "ds_read_b128 v[140:143], v149 offset:1536\n\t"
      "v_add_u32 v149, 2048, v149\n\t"
      "ds_write_b128 v4, v[96:99] offset:16384\n\t"
      "s_add_u32 s46, s46, 32768\n\t"
      "s_cmp_lt_u32 s46, 98304\n\t"
      "ds_write_b128 v4, v[100:103] offset:17408\n\t"
      "s_cselect_b32 s46, s46, 0\n\t"
      "s_add_u32 s41, s41, 128\n\t"
      "ds_write_b128 v4, v[104:107] offset:18432\n\t"
      "s_add_u32 m0, s16, s46\n\t"
      "s_add_u32 s45, m0, 4096\n\t"
      "ds_write_b128 v4, v[108:111] offset:19456\n\t"
      "s_waitcnt vmcnt(8)\n\t"
      "ds_add_u32 v5, v148 offset:4\n\t"
      "buffer_load_dwordx4 v120, s[8:11], s41 offen offset:0  lds\n\t"
      "buffer_load_dwordx4 v121, s[8:11], s41 offen offset:1024  lds\n\t"
      "buffer_load_dwordx4 v122, s[8:11], s41 offen offset:2048  lds\n\t"
      "buffer_load_dwordx4 v123, s[8:11], s41 offen offset:3072  lds\n\t"
      "s_mov_b32 m0, s45\n\t"
      "s_nop 0\n\t"
      "buffer_load_dwordx4 v124, s[8:11], s41 offen offset:0  lds\n\t"
      "buffer_load_dwordx4 v125, s[8:11], s41 offen offset:1024  lds\n\t"
      "buffer_load_dwordx4 v126, s[8:11], s41 offen offset:2048  lds\n\t"
      "buffer_load_dwordx4 v127, s[8:11], s41 offen offset:3072  lds\n\t"
      "s_add_u32 s47, s47, 4\n\t"
      "s_add_u32 s44, s44, 2\n\t"
      "s_cmp_lt_u32 s44, 14\n\t"
      "s_cbranch_scc1 Lp_loopf_%=\n\t"
      "s_add_u32 s42, s42, 917504\n\t"
      "buffer_load_dwordx4 v[64:67], v128, s[12:15], s42 offen \n\t"
      "s_waitcnt vmcnt(13) lgkmcnt(0)\n\t"
      "v_fma_mix_f32 v112, v32, v136, v140 op_sel_hi:[1,0,0]\n\t"
      "v_fma_mix_f32 v113, v33, v137, v141 op_sel_hi:[1,0,0]\n\t"
      "v_fma_mix_f32 v114, v34, v138, v142 op_sel_hi:[1,0,0]\n\t"
      "v_fma_mix_f32 v115, v35, v139, v143 op_sel_hi:[1,0,0]\n\t"
      "buffer_load_dwordx4 v[68:71], v129, s[12:15], s42 offen \n\t"
      "v_fma_mix_f32 v116, v36, v136, v140 op_sel_hi:[1,0,0]\n\t"
      "v_fma_mix_f32 v117, v37, v137, v141 op_sel_hi:[1,0,0]\n\t"
      "v_fma_mix_f32 v118, v38, v138, v142 op_sel_hi:[1,0,0]\n\t"
      "v_fma_mix_f32 v119, v39, v139, v143 op_sel_hi:[1,0,0]\n\t"
      "buffer_load_dwordx4 v[72:75], v130, s[12:15], s42 offen \n\t"
      "v_cvt_pk_bf16_f32 v96, v112, v116\n\t"
      "v_cvt_pk_bf16_f32 v100, v113, v117\n\t"
      "v_cvt_pk_bf16_f32 v104, v114, v118\n\t"
      "v_cvt_pk_bf16_f32 v108, v115, v119\n\t"
      "v_fma_mix_f32 v112, v40, v136, v140 op_sel_hi:[1,0,0]\n\t"
      "v_fma_mix_f32 v113, v41, v137, v141 op_sel_hi:[1,0,0]\n\t"
      "v_fma_mix_f32 v114, v42, v138, v142 op_sel_hi:[1,0,0]\n\t"
      "v_fma_mix_f32 v115, v43, v139, v143 op_sel_hi:[1,0,0]\n\t"
      "buffer_load_dwordx4 v[76:79], v131, s[12:15], s42 offen \n\t"
      "v_fma_mix_f32 v116, v44, v136, v140 op_sel_hi:[1,0,0]\n\t"
      "v_fma_mix_f32 v117, v45, v137, v141 op_sel_hi:[1,0,0]\n\t"
      "v_fma_mix_f32 v118, v46, v138, v142 op_sel_hi:[1,0,0]\n\t"
      "v_fma_mix_f32 v119, v47, v139, v143 op_sel_hi:[1,0,0]\n\t"
      "buffer_load_dwordx4 v[80:83], v132, s[12:15], s42 offen \n\t"
      "v_cvt_pk_bf16_f32 v97, v112, v116\n\t"
      "v_cvt_pk_bf16_f32 v101, v113, v117\n\t"
      "v_cvt_pk_bf16_f32 v105, v114, v118\n\t"
      "v_cvt_pk_bf16_f32 v109, v115, v119\n\t"
      "s_waitcnt vmcnt(13)\n\t"
      "v_fma_mix_f32 v112, v48, v136, v140 op_sel_hi:[1,0,0]\n\t"
      "v_fma_mix_f32 v113, v49, v137, v141 op_sel_hi:[1,0,0]\n\t"
      "v_fma_mix_f32 v114, v50, v138, v142 op_sel_hi:[1,0,0]\n\t"
      "v_fma_mix_f32 v115, v51, v139, v143 op_sel_hi:[1,0,0]\n\t"
      "buffer_load_dwordx4 v[84:87], v133, s[12:15], s42 offen \n\t"
      "v_fma_mix_f32 v116, v52, v136, v140 op_sel_hi:[1,0,0]\n\t"
      "v_fma_mix_f32 v117, v53, v137, v141 op_sel_hi:[1,0,0]\n\t"
      "v_fma_mix_f32 v118, v54, v138, v142 op_sel_hi:[1,0,0]\n\t"
      "v_fma_mix_f32 v119, v55, v139, v143 op_sel_hi:[1,0,0]\n\t"
      "buffer_load_dwordx4 v[88:91], v134, s[12:15], s42 offen \n\t"
      "v_cvt_pk_bf16_f32 v98, v112, v116\n\t"
      "v_cvt_pk_bf16_f32 v102, v113, v117\n\t"
      "v_cvt_pk_bf16_f32 v106, v114, v118\n\t"
      "v_cvt_pk_bf16_f32 v110, v115, v119\n\t"
      "ds_read_b32 v144, v6 offset:8\n\t"
      "v_fma_mix_f32 v112, v56, v136, v140 op_sel_hi:[1,0,0]\n\t"
      "v_fma_mix_f32 v113, v57, v137, v141 op_sel_hi:[1,0,0]\n\t"
      "v_fma_mix_f32 v114, v58, v138, v142 op_sel_hi:[1,0,0]\n\t"
      "v_fma_mix_f32 v115, v59, v139, v143 op_sel_hi:[1,0,0]\n\t"
      "buffer_load_dwordx4 v[92:95], v135, s[12:15], s42 offen \n\t"
      "v_fma_mix_f32 v116, v60, v136, v140 op_sel_hi:[1,0,0]\n\t"
      "v_fma_mix_f32 v117, v61, v137, v141 op_sel_hi:[1,0,0]\n\t"
      "v_fma_mix_f32 v118, v62, v138, v142 op_sel_hi:[1,0,0]\n\t"
      "v_fma_mix_f32 v119, v63, v139, v143 op_sel_hi:[1,0,0]\n\t"
      "v_cvt_pk_bf16_f32 v99, v112, v116\n\t"
      "v_cvt_pk_bf16_f32 v103, v113, v117\n\t"
      "v_cvt_pk_bf16_f32 v107, v114, v118\n\t"
      "v_cvt_pk_bf16_f32 v111, v115, v119\n\t"
      "s_waitcnt lgkmcnt(0)\n\t"
      "Lpc_ft0_%=:\n\t"
      "v_readfirstlane_b32 s48, v144\n\t"
      "s_nop 1\n\t"
      "s_cmp_ge_u32 s48, s47\n\t"
      "s_cbranch_scc0 Lpw_ft0_%=\n\t"
      "Lpg_ft0_%=:\n\t"
      "ds_write_b128 v4, v[96:99] offset:0\n\t"
      "s_add_u32 s46, s46, 32768\n\t"
      "s_cmp_lt_u32 s46, 98304\n\t"
      "ds_write_b128 v4, v[100:103] offset:1024\n\t"
      "s_cselect_b32 s46, s46, 0\n\t"
      "s_add_u32 s41, s41, 128\n\t"
      "ds_write_b128 v4, v[104:107] offset:2048\n\t"
      "s_add_u32 m0, s16, s46\n\t"
      "s_add_u32 s45, m0, 4096\n\t"
      "ds_write_b128 v4, v[108:111] offset:3072\n\t"
      "s_waitcnt vmcnt(8)\n\t"
      "ds_add_u32 v5, v148 offset:0\n\t"
      "buffer_load_dwordx4 v120, s[8:11], s41 offen offset:0  lds\n\t"
      "buffer_load_dwordx4 v121, s[8:11], s41 offen offset:1024  lds\n\t"
      "buffer_load_dwordx4 v122, s[8:11], s41 offen offset:2048  lds\n\t"
      "buffer_load_dwordx4 v123, s[8:11], s41 offen offset:3072  lds\n\t"
      "s_mov_b32 m0, s45\n\t"
      "s_nop 0\n\t"
      "buffer_load_dwordx4 v124, s[8:11], s41 offen offset:0  lds\n\t"
      "buffer_load_dwordx4 v125, s[8:11], s41 offen offset:1024  lds\n\t"
      "buffer_load_dwordx4 v126, s[8:11], s41 offen offset:2048  lds\n\t"
      "buffer_load_dwordx4 v127, s[8:11], s41 offen offset:3072  lds\n\t"
      "s_waitcnt vmcnt(12) lgkmcnt(0)\n\t"
      "v_fma_mix_f32 v112, v64, v136, v140 op_sel_hi:[1,0,0]\n\t"
      "v_fma_mix_f32 v113, v65, v137, v141 op_sel_hi:[1,0,0]\n\t"
      "v_fma_mix_f32 v114, v66, v138, v142 op_sel_hi:[1,0,0]\n\t"
      "v_fma_mix_f32 v115, v67, v139, v143 op_sel_hi:[1,0,0]\n\t"
      "v_fma_mix_f32 v116, v68, v136, v140 op_sel_hi:[1,0,0]\n\t"
      "v_fma_mix_f32 v117, v69, v137, v141 op_sel_hi:[1,0,0]\n\t"
      "v_fma_mix_f32 v118, v70, v138, v142 op_sel_hi:[1,0,0]\n\t"
      "v_fma_mix_f32 v119, v71, v139, v143 op_sel_hi:[1,0,0]\n\t"
      "v_cvt_pk_bf16_f32 v96, v112, v116\n\t"
      "v_cvt_pk_bf16_f32 v100, v113, v117\n\t"
      "v_cvt_pk_bf16_f32 v104, v114, v118\n\t"
      "v_cvt_pk_bf16_f32 v108, v115, v119\n\t"
      "v_fma_mix_f32 v112, v72, v136, v140 op_sel_hi:[1,0,0]\n\t"
      "v_fma_mix_f32 v113, v73, v137, v141 op_sel_hi:[1,0,0]\n\t"
      "v_fma_mix_f32 v114, v74, v138, v142 op_sel_hi:[1,0,0]\n\t"
      "v_fma_mix_f32 v115, v75, v139, v143 op_sel_hi:[1,0,0]\n\t"
      "v_fma_mix_f32 v116, v76, v136, v140 op_sel_hi:[1,0,0]\n\t"
      "v_fma_mix_f32 v117, v77, v137, v141 op_sel_hi:[1,0,0]\n\t"
      "v_fma_mix_f32 v118, v78, v138, v142 op_sel_hi:[1,0,0]\n\t"
      "v_fma_mix_f32 v119, v79, v139, v143 op_sel_hi:[1,0,0]\n\t"
      "v_cvt_pk_bf16_f32 v97, v112, v116\n\t"
      "v_cvt_pk_bf16_f32 v101, v113, v117\n\t"
      "v_cvt_pk_bf16_f32 v105, v114, v118\n\t"
      "v_cvt_pk_bf16_f32 v109, v115, v119\n\t"
      "s_waitcnt vmcnt(8)\n\t"
      "v_fma_mix_f32 v112, v80, v136, v140 op_sel_hi:[1,0,0]\n\t"
      "v_fma_mix_f32 v113, v81, v137, v141 op_sel_hi:[1,0,0]\n\t"
      "v_fma_mix_f32 v114, v82, v138, v142 op_sel_hi:[1,0,0]\n\t"
      "v_fma_mix_f32 v115, v83, v139, v143 op_sel_hi:[1,0,0]\n\t"
      "v_fma_mix_f32 v116, v84, v136, v140 op_sel_hi:[1,0,0]\n\t"
      "v_fma_mix_f32 v117, v85, v137, v141 op_sel_hi:[1,0,0]\n\t"
      "v_fma_mix_f32 v118, v86, v138, v142 op_sel_hi:[1,0,0]\n\t"
      "v_fma_mix_f32 v119, v87, v139, v143 op_sel_hi:[1,0,0]\n\t"
      "v_cvt_pk_bf16_f32 v98, v112, v116\n\t"
      "v_cvt_pk_bf16_f32 v102, v113, v117\n\t"
      "v_cvt_pk_bf16_f32 v106, v114, v118\n\t"
      "v_cvt_pk_bf16_f32 v110, v115, v119\n\t"
      "ds_read_b32 v144, v6 offset:12\n\t"
      "v_fma_mix_f32 v112, v88, v136, v140 op_sel_hi:[1,0,0]\n\t"
      "v_fma_mix_f32 v113, v89, v137, v141 op_sel_hi:[1,0,0]\n\t"
      "v_fma_mix_f32 v114, v90, v138, v142 op_sel_hi:[1,0,0]\n\t"
      "v_fma_mix_f32 v115, v91, v139, v143 op_sel_hi:[1,0,0]\n\t"
      "v_fma_mix_f32 v116, v92, v136, v140 op_sel_hi:[1,0,0]\n\t"
      "v_fma_mix_f32 v117, v93, v137, v141 op_sel_hi:[1,0,0]\n\t"
      "v_fma_mix_f32 v118, v94, v138, v142 op_sel_hi:[1,0,0]\n\t"
      "v_fma_mix_f32 v119, v95, v139, v143 op_sel_hi:[1,0,0]\n\t"
      "v_cvt_pk_bf16_f32 v99, v112, v116\n\t"
      "v_cvt_pk_bf16_f32 v103, v113, v117\n\t"
      "v_cvt_pk_bf16_f32 v107, v114, v118\n\t"
      "v_cvt_pk_bf16_f32 v111, v115, v119\n\t"
      "s_waitcnt lgkmcnt(0)\n\t"
      "Lpc_ft1_%=:\n\t"
      "v_readfirstlane_b32 s48, v144\n\t"
      "s_nop 1\n\t"
      "s_cmp_ge_u32 s48, s47\n\t"
      "s_cbranch_scc0 Lpw_ft1_%=\n\t"
      "Lpg_ft1_%=:\n\t"
      "ds_write_b128 v4, v[96:99] offset:16384\n\t"
      "ds_write_b128 v4, v[100:103] offset:17408\n\t"
      "ds_write_b128 v4, v[104:107] offset:18432\n\t"
      "ds_write_b128 v4, v[108:111] offset:19456\n\t"
      "s_waitcnt vmcnt(0)\n\t"
      "ds_add_u32 v5, v148 offset:4\n\t"
      "s_add_u32 s47, s47, 4\n\t"
      "s_branch Lp_end_%=\n\t"
      "Lpw_f0_%=:\n\t"
      "ds_read_b32 v144, v6 offset:8\n\t"
      "s_add_u32 s39, s39, 1\n\t"
      "s_cmp_lt_u32 s39, 0x4000000\n\t"
      "s_waitcnt lgkmcnt(0)\n\t"
      "s_cbranch_scc1 Lpc_f0_%=\n\t"
      "s_branch Lpg_f0_%=\n\t"
      "Lpw_f1_%=:\n\t"
      "ds_read_b32 v144, v6 offset:12\n\t"
      "s_add_u32 s39, s39, 1\n\t"
      "s_cmp_lt_u32 s39, 0x4000000\n\t"
      "s_waitcnt lgkmcnt(0)\n\t"
      "s_cbranch_scc1 Lpc_f1_%=\n\t"
      "s_branch Lpg_f1_%=\n\t"
      "Lpw_ft0_%=:\n\t"
      "ds_read_b32 v144, v6 offset:8\n\t"
      "s_add_u32 s39, s39, 1\n\t"
      "s_cmp_lt_u32 s39, 0x4000000\n\t"
      "s_waitcnt lgkmcnt(0)\n\t"
      "s_cbranch_scc1 Lpc_ft0_%=\n\t"
      "s_branch Lpg_ft0_%=\n\t"
      "Lpw_ft1_%=:\n\t"
      "ds_read_b32 v144, v6 offset:12\n\t"
      "s_add_u32 s39, s39, 1\n\t"
      "s_cmp_lt_u32 s39, 0x4000000\n\t"
      "s_waitcnt lgkmcnt(0)\n\t"
      "s_cbranch_scc1 Lpc_ft1_%=\n\t"
      "s_branch Lpg_ft1_%=\n\t"
      "Lp_end_%=:\n\t"
      "s_waitcnt vmcnt(0) lgkmcnt(0)\n\t"
      "s_endpgm\n\t"
      "Lcons_%=:\n\t"
      "s_setprio 3\n\t"
      "s_sub_u32 s24, s24, 4\n\t"
      "s_mov_b32 s14, 57344\n\t"
      "s_mov_b32 s15, 0x20000\n\t"
      "v_lshrrev_b32 v15, 5, v1\n\t"
      "v_and_b32 v16, 31, v1\n\t"
      "s_mul_i32 s27, s24, 14336\n\t"
      "s_lshl_b32 s29, s26, 8\n\t"
      "s_add_u32 s27, s27, s29\n\t"
      "v_mul_u32_u24 v8, 7168, v15\n\t"
      "v_lshl_add_u32 v8, v16, 3, v8\n\t"
      "s_mov_b32 s18, 57344\n\t"
      "s_mov_b32 s19, 0x20000\n\t"
      "s_waitcnt lgkmcnt(0)\n\t"
      "s_mov_b32 s12, s4\n\t"
      "s_and_b32 s13, s5, 0xffff\n\t"
      "s_mov_b32 s16, s6\n\t"
      "s_and_b32 s17, s7, 0xffff\n\t"
      "buffer_load_dwordx2 v[32:33], v8, s[12:15], s27 offen\n\t"
      "buffer_load_dwordx2 v[34:35], v8, s[16:19], s27 offen\n\t"
      "s_lshl_b32 s27, s24, 12\n\t"
      "s_add_u32 s27, s27, 131072\n\t"
      "v_lshl_add_u32 v9, v15, 11, s27\n\t"
      "v_lshl_add_u32 v9, v16, 4, v9\n\t"
      "v_mov_b32 v7, 147456\n\t"
      "v_lshl_add_u32 v17, v1, 2, v7\n\t"
      "v_add_u32 v17, 64, v17\n\t"
      "v_cmp_eq_u32 vcc, 0, v1\n\t"
      "v_mov_b32 v18, 147464\n\t"
      "s_lshl_b32 s27, s24, 2\n\t"
      "s_add_u32 s27, s27, 147472\n\t"
      "v_mov_b32 v19, s27\n\t"
      "v_cndmask_b32 v6, v17, v18, vcc\n\t"
      "v_cndmask_b32 v10, v17, v19, vcc\n\t"
      "v_and_b32 v15, 15, v1\n\t"
      "v_lshrrev_b32 v16, 4, v1\n\t"
      "s_lshl_b32 s27, s24, 13\n\t"
      "v_and_b32 v17, 6, v15\n\t"
      "v_xor_b32 v17, v17, v16\n\t"
      "v_lshl_add_u32 v2, v15, 7, s27\n\t"
      "v_lshl_add_u32 v2, v17, 4, v2\n\t"
      "v_xor_b32 v3, 64, v2\n\t"
      "v_bfe_u32 v17, v1, 3, 1\n\t"
      "v_lshlrev_b32 v17, 1, v17\n\t"
      "v_xor_b32 v17, v17, v1\n\t"
      "v_lshlrev_b32 v4, 4, v17\n\t"
      "v_add_u32 v4, 98304, v4\n\t"
      "s_lshl_b32 s29, s25, 2\n\t"
      "s_add_u32 s29, s29, s24\n\t"
      "s_mul_i32 s29, s29, 458752\n\t"
      "s_lshl_b32 s34, s26, 8\n\t"
      "s_add_u32 s29, s29, s34\n\t"
      "s_add_u32 s8, s32, s29\n\t"
      "s_addc_u32 s9, s33, 0\n\t"
      "s_and_b32 s9, s9, 0xffff\n\t"
      "s_sub_u32 s10, 458752, s34\n\t"
      "s_mov_b32 s11, 0x20000\n\t"
      "v_mul_u32_u24 v5, 7168, v16\n\t"
      "v_lshl_add_u32 v5, v15, 4, v5\n\t"
      "s_add_u32 s27, s27, 32768\n\t"
      "v_and_b32 v17, 7, v15\n\t"
      "v_xor_b32 v17, v17, v16\n\t"
      "v_lshl_add_u32 v11, v15, 8, s27\n\t"
      "v_lshl_add_u32 v11, v17, 4, v11\n\t"
      "v_xor_b32 v12, 64, v11\n\t"
      "v_xor_b32 v17, v15, v16\n\t"
      "v_lshl_add_u32 v13, v16, 8, s27\n\t"
      "v_lshl_add_u32 v13, v17, 4, v13\n\t"
      "v_xor_b32 v14, 64, v13\n\t"
      "v_add_u32 v14, 1024, v14\n\t"
      "v_mov_b32 v20, 0\n\t"
      "v_mov_b32 v21, 0\n\t"
      "v_mov_b32 v22, 0\n\t"
      "v_mov_b32 v23, 0\n\t"
      "s_mov_b64 exec, 1\n\t"
      "ds_write_b128 v7, v[20:23]\n\t"
      "s_mov_b64 exec, -1\n\t"
      "s_setprio 0\n\t"
      "v_mov_b32 v128, 0\n\t"
      "v_mov_b32 v129, 0\n\t"
      "v_mov_b32 v130, 0\n\t"
      "v_mov_b32 v131, 0\n\t"
      "v_mov_b32 v132, 0\n\t"
      "v_mov_b32 v133, 0\n\t"
      "v_mov_b32 v134, 0\n\t"
      "v_mov_b32 v135, 0\n\t"
      "v_mov_b32 v136, 0\n\t"
      "v_mov_b32 v137, 0\n\t"
      "v_mov_b32 v138, 0\n\t"
      "v_mov_b32 v139, 0\n\t"
      "v_mov_b32 v140, 0\n\t"
      "v_mov_b32 v141, 0\n\t"
      "v_mov_b32 v142, 0\n\t"
      "v_mov_b32 v143, 0\n\t"
      "v_mov_b32 v144, 0\n\t"
      "v_mov_b32 v145, 0\n\t"
      "v_mov_b32 v146, 0\n\t"
      "v_mov_b32 v147, 0\n\t"
      "v_mov_b32 v148, 0\n\t"
      "v_mov_b32 v149, 0\n\t"
      "v_mov_b32 v150, 0\n\t"
      "v_mov_b32 v151, 0\n\t"
      "v_mov_b32 v152, 0\n\t"
      "v_mov_b32 v153, 0\n\t"
      "v_mov_b32 v154, 0\n\t"
      "v_mov_b32 v155, 0\n\t"
      "v_mov_b32 v156, 0\n\t"
      "v_mov_b32 v157, 0\n\t"
      "v_mov_b32 v158, 0\n\t"
      "v_mov_b32 v159, 0\n\t"
      "v_mov_b32 v160, 0\n\t"
      "v_mov_b32 v161, 0\n\t"
      "v_mov_b32 v162, 0\n\t"
      "v_mov_b32 v163, 0\n\t"
      "v_mov_b32 v164, 0\n\t"
      "v_mov_b32 v165, 0\n\t"
      "v_mov_b32 v166, 0\n\t"
      "v_mov_b32 v167, 0\n\t"
      "v_mov_b32 v168, 0\n\t"
      "v_mov_b32 v169, 0\n\t"
      "v_mov_b32 v170, 0\n\t"
      "v_mov_b32 v171, 0\n\t"
      "v_mov_b32 v172, 0\n\t"
      "v_mov_b32 v173, 0\n\t"
      "v_mov_b32 v174, 0\n\t"
      "v_mov_b32 v175, 0\n\t"
      "v_mov_b32 v176, 0\n\t"
      "v_mov_b32 v177, 0\n\t"
      "v_mov_b32 v178, 0\n\t"
      "v_mov_b32 v179, 0\n\t"
      "v_mov_b32 v180, 0\n\t"
      "v_mov_b32 v181, 0\n\t"
      "v_mov_b32 v182, 0\n\t"
      "v_mov_b32 v183, 0\n\t"
      "v_mov_b32 v184, 0\n\t"
      "v_mov_b32 v185, 0\n\t"
      "v_mov_b32 v186, 0\n\t"
      "v_mov_b32 v187, 0\n\t"
      "v_mov_b32 v188, 0\n\t"
      "v_mov_b32 v189, 0\n\t"
      "v_mov_b32 v190, 0\n\t"
      "v_mov_b32 v191, 0\n\t"
      "v_mov_b32 v192, 0\n\t"
      "v_mov_b32 v193, 0\n\t"
      "v_mov_b32 v194, 0\n\t"
      "v_mov_b32 v195, 0\n\t"
      "v_mov_b32 v196, 0\n\t"
      "v_mov_b32 v197, 0\n\t"
      "v_mov_b32 v198, 0\n\t"
      "v_mov_b32 v199, 0\n\t"
      "v_mov_b32 v200, 0\n\t"
      "v_mov_b32 v201, 0\n\t"
      "v_mov_b32 v202, 0\n\t"
      "v_mov_b32 v203, 0\n\t"
      "v_mov_b32 v204, 0\n\t"
      "v_mov_b32 v205, 0\n\t"
      "v_mov_b32 v206, 0\n\t"
      "v_mov_b32 v207, 0\n\t"
      "v_mov_b32 v208, 0\n\t"
      "v_mov_b32 v209, 0\n\t"
      "v_mov_b32 v210, 0\n\t"
      "v_mov_b32 v211, 0\n\t"
      "v_mov_b32 v212, 0\n\t"
      "v_mov_b32 v213, 0\n\t"
      "v_mov_b32 v214, 0\n\t"
      "v_mov_b32 v215, 0\n\t"
      "v_mov_b32 v216, 0\n\t"
      "v_mov_b32 v217, 0\n\t"
      "v_mov_b32 v218, 0\n\t"
      "v_mov_b32 v219, 0\n\t"
      "v_mov_b32 v220, 0\n\t"
      "v_mov_b32 v221, 0\n\t"
      "v_mov_b32 v222, 0\n\t"
      "v_mov_b32 v223, 0\n\t"
      "v_mov_b32 v224, 0\n\t"
      "v_mov_b32 v225, 0\n\t"
      "v_mov_b32 v226, 0\n\t"
      "v_mov_b32 v227, 0\n\t"
      "v_mov_b32 v228, 0\n\t"
      "v_mov_b32 v229, 0\n\t"
      "v_mov_b32 v230, 0\n\t"
      "v_mov_b32 v231, 0\n\t"
      "v_mov_b32 v232, 0\n\t"
      "v_mov_b32 v233, 0\n\t"
      "v_mov_b32 v234, 0\n\t"
      "v_mov_b32 v235, 0\n\t"
      "v_mov_b32 v236, 0\n\t"
      "v_mov_b32 v237, 0\n\t"
      "v_mov_b32 v238, 0\n\t"
      "v_mov_b32 v239, 0\n\t"
      "v_mov_b32 v240, 0\n\t"
      "v_mov_b32 v241, 0\n\t"
      "v_mov_b32 v242, 0\n\t"
      "v_mov_b32 v243, 0\n\t"
      "v_mov_b32 v244, 0\n\t"
      "v_mov_b32 v245, 0\n\t"
      "v_mov_b32 v246, 0\n\t"
      "v_mov_b32 v247, 0\n\t"
      "v_mov_b32 v248, 0\n\t"
      "v_mov_b32 v249, 0\n\t"
      "v_mov_b32 v250, 0\n\t"
      "v_mov_b32 v251, 0\n\t"
      "v_mov_b32 v252, 0\n\t"
      "v_mov_b32 v253, 0\n\t"
      "v_mov_b32 v254, 0\n\t"
      "v_mov_b32 v255, 0\n\t"
      "v_mov_b32 v18, 1\n\t"
      "s_mov_b32 s42, 0x4b800000\n\t"
      "s_waitcnt vmcnt(0)\n\t"
      "s_setprio 2\n\t"
      "v_lshlrev_b32 v36, 16, v32\n\t"
      "v_and_b32 v37, 0xffff0000, v32\n\t"
      "v_lshlrev_b32 v38, 16, v33\n\t"
      "v_and_b32 v39, 0xffff0000, v33\n\t"
      "v_lshlrev_b32 v40, 16, v34\n\t"
      "v_and_b32 v41, 0xffff0000, v34\n\t"
      "v_lshlrev_b32 v42, 16, v35\n\t"
      "v_and_b32 v43, 0xffff0000, v35\n\t"
      "v_mul_f32 v44, s42, v36\n\t"
      "v_mul_f32 v45, s42, v37\n\t"
      "v_mul_f32 v46, s42, v38\n\t"
      "v_mul_f32 v47, s42, v39\n\t"
      "v_mul_f32 v48, -v40, v36\n\t"
      "v_mul_f32 v49, -v41, v37\n\t"
      "v_mul_f32 v50, -v42, v38\n\t"
      "v_mul_f32 v51, -v43, v39\n\t"
      "ds_write_b128 v9, v[44:47] offset:1024\n\t"
      "ds_write_b128 v9, v[48:51] offset:1536\n\t"
      "ds_write_b128 v9, v[36:39]\n\t"
      "ds_write_b128 v9, v[40:43] offset:512\n\t"
      "v_bfe_u32 v56, v36, 23, 8\n\t"
      "v_bfe_u32 v57, v37, 23, 8\n\t"
      "v_bfe_u32 v58, v38, 23, 8\n\t"
      "v_bfe_u32 v59, v39, 23, 8\n\t"
      "v_rndne_f32 v52, v40\n\t"
      "v_rndne_f32 v53, v41\n\t"
      "v_rndne_f32 v54, v42\n\t"
      "v_rndne_f32 v55, v43\n\t"
      "v_min3_u32 v61, v56, v57, v58\n\t"
      "v_max3_u32 v62, v56, v57, v58\n\t"
      "v_cmp_eq_f32 s[44:45], v52, v40\n\t"
      "v_cmp_eq_f32 s[46:47], v53, v41\n\t"
      "v_cmp_eq_f32 s[48:49], v54, v42\n\t"
      "v_cmp_eq_f32 vcc, v55, v43\n\t"
      "v_max3_f32 v60, abs(v40), abs(v41), abs(v42)\n\t"
      "v_min_u32 v61, v61, v59\n\t"
      "v_max_u32 v62, v62, v59\n\t"
      "v_max_f32 v60, v60, abs(v43)\n\t"
      "s_and_b64 s[44:45], s[44:45], s[46:47]\n\t"
      "s_and_b64 s[48:49], s[48:49], vcc\n\t"
      "v_cmp_le_u32 s[46:47], 63, v61\n\t"
      "v_cmp_ge_u32 vcc, 0xbe, v62\n\t"
      "s_and_b64 s[44:45], s[44:45], s[48:49]\n\t"
      "s_nop 1\n\t"
      "s_and_b64 s[46:47], s[46:47], vcc\n\t"
      "v_cmp_ge_f32 vcc, 0x43000000, v60\n\t"
      "s_and_b64 s[44:45], s[44:45], s[46:47]\n\t"
      "s_nop 1\n\t"
      "s_and_b64 s[44:45], s[44:45], vcc\n\t"
      "s_cmp_eq_u64 s[44:45], -1\n\t"
      "s_cselect_b32 s44, 1, 0\n\t"
      "v_mov_b32 v60, s44\n\t"
      "ds_write_b32 v10, v60\n\t"
      "s_waitcnt lgkmcnt(0)\n\t"
      "s_barrier\n\t"
      "v_mov_b32 v16, v2\n\t"
      "v_mov_b32 v17, v3\n\t"
      "s_mov_b32 s36, 0\n\t"
      "s_mov_b32 s35, 0\n\t"
      "s_mov_b32 s37, 4\n\t"
      "s_mov_b32 s40, 0\n\t"
      "Lci_%=:\n\t"
      "ds_read_b32 v20, v7 offset:0\n\t"
      "s_add_u32 s35, s35, 1\n\t"
      "s_cmp_ge_u32 s35, 0x4000000\n\t"
      "s_cbranch_scc1 Lcj_%=\n\t"
      "s_waitcnt lgkmcnt(0)\n\t"
      "v_readfirstlane_b32 s39, v20\n\t"
      "s_nop 1\n\t"
      "s_cmp_ge_u32 s39, s37\n\t"
      "s_cbranch_scc0 Lci_%=\n\t"
      "Lcj_%=:\n\t"
      "ds_read_b128 v[64:67], v4 offset:0\n\t"
      "ds_read_b128 v[32:35], v16 offset:0\n\t"
      "ds_read_b128 v[36:39], v16 offset:2048\n\t"
      "ds_read_b128 v[40:43], v16 offset:4096\n\t"
      "ds_read_b128 v[44:47], v16 offset:6144\n\t"
      "ds_read_b128 v[68:71], v4 offset:1024\n\t"
      "ds_read_b128 v[72:75], v4 offset:2048\n\t"
      "ds_read_b128 v[76:79], v4 offset:3072\n\t"
      "ds_read_b128 v[80:83], v4 offset:4096\n\t"
      "ds_read_b128 v[84:87], v4 offset:5120\n\t"
      "ds_read_b128 v[88:91], v4 offset:6144\n\t"
      "ds_read_b128 v[92:95], v4 offset:7168\n\t"
      "s_waitcnt lgkmcnt(0)\n\t"
      "Lc_loop_%=:\n\t"
      "v_mfma_f32_16x16x32_bf16 v[128:131], v[64:67], v[32:35], v[128:131]\n\t"
      "ds_read_b128 v[96:99], v4 offset:8192\n\t"
      "s_add_u32 s36, s36, 32768\n\t"
      "v_mfma_f32_16x16x32_bf16 v[132:135], v[64:67], v[36:39], v[132:135]\n\t"
      "ds_read_b128 v[48:51], v17 offset:0\n\t"
      "s_cmp_lt_u32 s36, 98304\n\t"
      "s_cselect_b32 s36, s36, 0\n\t"
      "v_mfma_f32_16x16x32_bf16 v[136:139], v[64:67], v[40:43], v[136:139]\n\t"
      "v_add_u32 v16, s36, v2\n\t"
      "v_mfma_f32_16x16x32_bf16 v[140:143], v[64:67], v[44:47], v[140:143]\n\t"
      "ds_read_b128 v[52:55], v17 offset:2048\n\t"
      "v_mfma_f32_16x16x32_bf16 v[144:147], v[68:71], v[32:35], v[144:147]\n\t"
      "ds_read_b128 v[56:59], v17 offset:4096\n\t"
      "v_mfma_f32_16x16x32_bf16 v[148:151], v[68:71], v[36:39], v[148:151]\n\t"
      "v_mfma_f32_16x16x32_bf16 v[152:155], v[68:71], v[40:43], v[152:155]\n\t"
      "ds_read_b128 v[60:63], v17 offset:6144\n\t"
      "v_mfma_f32_16x16x32_bf16 v[156:159], v[68:71], v[44:47], v[156:159]\n\t"
      "ds_read_b128 v[100:103], v4 offset:9216\n\t"
      "v_mfma_f32_16x16x32_bf16 v[160:163], v[72:75], v[32:35], v[160:163]\n\t"
      "v_mfma_f32_16x16x32_bf16 v[164:167], v[72:75], v[36:39], v[164:167]\n\t"
      "ds_read_b128 v[104:107], v4 offset:10240\n\t"
      "v_mfma_f32_16x16x32_bf16 v[168:171], v[72:75], v[40:43], v[168:171]\n\t"
      "ds_read_b128 v[108:111], v4 offset:11264\n\t"
      "v_mfma_f32_16x16x32_bf16 v[172:175], v[72:75], v[44:47], v[172:175]\n\t"
      "v_mfma_f32_16x16x32_bf16 v[176:179], v[76:79], v[32:35], v[176:179]\n\t"
      "ds_read_b128 v[112:115], v4 offset:12288\n\t"
      "v_mfma_f32_16x16x32_bf16 v[180:183], v[76:79], v[36:39], v[180:183]\n\t"
      "ds_read_b128 v[116:119], v4 offset:13312\n\t"
      "v_mfma_f32_16x16x32_bf16 v[184:187], v[76:79], v[40:43], v[184:187]\n\t"
      "v_mfma_f32_16x16x32_bf16 v[188:191], v[76:79], v[44:47], v[188:191]\n\t"
      "ds_read_b128 v[120:123], v4 offset:14336\n\t"
      "v_mfma_f32_16x16x32_bf16 v[192:195], v[80:83], v[32:35], v[192:195]\n\t"
      "ds_read_b128 v[124:127], v4 offset:15360\n\t"
      "v_mfma_f32_16x16x32_bf16 v[196:199], v[80:83], v[36:39], v[196:199]\n\t"
      "ds_add_u32 v6, v18 offset:0\n\t"
      "v_mfma_f32_16x16x32_bf16 v[200:203], v[80:83], v[40:43], v[200:203]\n\t"
      "v_add_u32 v17, s36, v3\n\t"
      "v_mfma_f32_16x16x32_bf16 v[204:207], v[80:83], v[44:47], v[204:207]\n\t"
      "v_mfma_f32_16x16x32_bf16 v[208:211], v[84:87], v[32:35], v[208:211]\n\t"
      "v_mfma_f32_16x16x32_bf16 v[212:215], v[84:87], v[36:39], v[212:215]\n\t"
      "v_mfma_f32_16x16x32_bf16 v[216:219], v[84:87], v[40:43], v[216:219]\n\t"
      "v_mfma_f32_16x16x32_bf16 v[220:223], v[84:87], v[44:47], v[220:223]\n\t"
      "v_mfma_f32_16x16x32_bf16 v[224:227], v[88:91], v[32:35], v[224:227]\n\t"
      "ds_read_b32 v20, v7 offset:4\n\t"
      "v_mfma_f32_16x16x32_bf16 v[228:231], v[88:91], v[36:39], v[228:231]\n\t"
      "v_mfma_f32_16x16x32_bf16 v[232:235], v[88:91], v[40:43], v[232:235]\n\t"
      "v_mfma_f32_16x16x32_bf16 v[236:239], v[88:91], v[44:47], v[236:239]\n\t"
      "v_mfma_f32_16x16x32_bf16 v[240:243], v[92:95], v[32:35], v[240:243]\n\t"
      "v_mfma_f32_16x16x32_bf16 v[244:247], v[92:95], v[36:39], v[244:247]\n\t"
      "s_waitcnt lgkmcnt(0)\n\t"
      "v_readfirstlane_b32 s39, v20\n\t"
      "v_mfma_f32_16x16x32_bf16 v[248:251], v[92:95], v[40:43], v[248:251]\n\t"
      "v_mfma_f32_16x16x32_bf16 v[252:255], v[92:95], v[44:47], v[252:255]\n\t"
      "s_cmp_ge_u32 s39, s37\n\t"
      "s_cbranch_scc0 Lcs1_%=\n\t"
      "Lcg1_%=:\n\t"
      "v_mfma_f32_16x16x32_bf16 v[128:131], v[96:99], v[48:51], v[128:131]\n\t"
      "ds_read_b128 v[64:67], v4 offset:16384\n\t"
      "v_mfma_f32_16x16x32_bf16 v[132:135], v[96:99], v[52:55], v[132:135]\n\t"
      "ds_read_b128 v[32:35], v16 offset:0\n\t"
      "v_mfma_f32_16x16x32_bf16 v[136:139], v[96:99], v[56:59], v[136:139]\n\t"
      "v_mfma_f32_16x16x32_bf16 v[140:143], v[96:99], v[60:63], v[140:143]\n\t"
      "ds_read_b128 v[36:39], v16 offset:2048\n\t"
      "v_mfma_f32_16x16x32_bf16 v[144:147], v[100:103], v[48:51], v[144:147]\n\t"
      "ds_read_b128 v[40:43], v16 offset:4096\n\t"
      "v_mfma_f32_16x16x32_bf16 v[148:151], v[100:103], v[52:55], v[148:151]\n\t"
      "v_mfma_f32_16x16x32_bf16 v[152:155], v[100:103], v[56:59], v[152:155]\n\t"
      "ds_read_b128 v[44:47], v16 offset:6144\n\t"
      "v_mfma_f32_16x16x32_bf16 v[156:159], v[100:103], v[60:63], v[156:159]\n\t"
      "ds_read_b128 v[68:71], v4 offset:17408\n\t"
      "v_mfma_f32_16x16x32_bf16 v[160:163], v[104:107], v[48:51], v[160:163]\n\t"
      "v_mfma_f32_16x16x32_bf16 v[164:167], v[104:107], v[52:55], v[164:167]\n\t"
      "ds_read_b128 v[72:75], v4 offset:18432\n\t"
      "v_mfma_f32_16x16x32_bf16 v[168:171], v[104:107], v[56:59], v[168:171]\n\t"
      "ds_read_b128 v[76:79], v4 offset:19456\n\t"
      "v_mfma_f32_16x16x32_bf16 v[172:175], v[104:107], v[60:63], v[172:175]\n\t"
      "v_mfma_f32_16x16x32_bf16 v[176:179], v[108:111], v[48:51], v[176:179]\n\t"
      "ds_read_b128 v[80:83], v4 offset:20480\n\t"
      "v_mfma_f32_16x16x32_bf16 v[180:183], v[108:111], v[52:55], v[180:183]\n\t"
      "ds_read_b128 v[84:87], v4 offset:21504\n\t"
      "v_mfma_f32_16x16x32_bf16 v[184:187], v[108:111], v[56:59], v[184:187]\n\t"
      "v_mfma_f32_16x16x32_bf16 v[188:191], v[108:111], v[60:63], v[188:191]\n\t"
      "ds_read_b128 v[88:91], v4 offset:22528\n\t"
      "v_mfma_f32_16x16x32_bf16 v[192:195], v[112:115], v[48:51], v[192:195]\n\t"
      "ds_read_b128 v[92:95], v4 offset:23552\n\t"
      "v_mfma_f32_16x16x32_bf16 v[196:199], v[112:115], v[52:55], v[196:199]\n\t"
      "v_mfma_f32_16x16x32_bf16 v[200:203], v[112:115], v[56:59], v[200:203]\n\t"
      "v_mfma_f32_16x16x32_bf16 v[204:207], v[112:115], v[60:63], v[204:207]\n\t"
      "v_mfma_f32_16x16x32_bf16 v[208:211], v[116:119], v[48:51], v[208:211]\n\t"
      "v_mfma_f32_16x16x32_bf16 v[212:215], v[116:119], v[52:55], v[212:215]\n\t"
      "v_mfma_f32_16x16x32_bf16 v[216:219], v[116:119], v[56:59], v[216:219]\n\t"
      "v_mfma_f32_16x16x32_bf16 v[220:223], v[116:119], v[60:63], v[220:223]\n\t"
      "v_mfma_f32_16x16x32_bf16 v[224:227], v[120:123], v[48:51], v[224:227]\n\t"
      "v_mfma_f32_16x16x32_bf16 v[228:231], v[120:123], v[52:55], v[228:231]\n\t"
      "v_mfma_f32_16x16x32_bf16 v[232:235], v[120:123], v[56:59], v[232:235]\n\t"
      "v_mfma_f32_16x16x32_bf16 v[236:239], v[120:123], v[60:63], v[236:239]\n\t"
      "v_mfma_f32_16x16x32_bf16 v[240:243], v[124:127], v[48:51], v[240:243]\n\t"
      "v_mfma_f32_16x16x32_bf16 v[244:247], v[124:127], v[52:55], v[244:247]\n\t"
      "v_mfma_f32_16x16x32_bf16 v[248:251], v[124:127], v[56:59], v[248:251]\n\t"
      "v_mfma_f32_16x16x32_bf16 v[252:255], v[124:127], v[60:63], v[252:255]\n\t"
      "s_waitcnt lgkmcnt(0)\n\t"
      "s_cmp_eq_u32 s40, 14\n\t"
      "s_cbranch_scc1 Lc_last_%=\n\t"
      "v_mfma_f32_16x16x32_bf16 v[128:131], v[64:67], v[32:35], v[128:131]\n\t"
      "ds_read_b128 v[96:99], v4 offset:24576\n\t"
      "s_add_u32 s36, s36, 32768\n\t"
      "v_mfma_f32_16x16x32_bf16 v[132:135], v[64:67], v[36:39], v[132:135]\n\t"
      "ds_read_b128 v[48:51], v17 offset:0\n\t"
      "s_cmp_lt_u32 s36, 98304\n\t"
      "s_cselect_b32 s36, s36, 0\n\t"
      "v_mfma_f32_16x16x32_bf16 v[136:139], v[64:67], v[40:43], v[136:139]\n\t"
      "v_add_u32 v16, s36, v2\n\t"
      "v_mfma_f32_16x16x32_bf16 v[140:143], v[64:67], v[44:47], v[140:143]\n\t"
      "ds_read_b128 v[52:55], v17 offset:2048\n\t"
      "v_mfma_f32_16x16x32_bf16 v[144:147], v[68:71], v[32:35], v[144:147]\n\t"
      "ds_read_b128 v[56:59], v17 offset:4096\n\t"
      "s_add_u32 s37, s37, 4\n\t"
      "s_min_u32 s37, s37, 32\n\t"
      "v_mfma_f32_16x16x32_bf16 v[148:151], v[68:71], v[36:39], v[148:151]\n\t"
      "s_add_u32 s40, s40, 2\n\t"
      "v_mfma_f32_16x16x32_bf16 v[152:155], v[68:71], v[40:43], v[152:155]\n\t"
      "ds_read_b128 v[60:63], v17 offset:6144\n\t"
      "v_mfma_f32_16x16x32_bf16 v[156:159], v[68:71], v[44:47], v[156:159]\n\t"
      "ds_read_b128 v[100:103], v4 offset:25600\n\t"
      "v_mfma_f32_16x16x32_bf16 v[160:163], v[72:75], v[32:35], v[160:163]\n\t"
      "v_mfma_f32_16x16x32_bf16 v[164:167], v[72:75], v[36:39], v[164:167]\n\t"
      "ds_read_b128 v[104:107], v4 offset:26624\n\t"
      "v_mfma_f32_16x16x32_bf16 v[168:171], v[72:75], v[40:43], v[168:171]\n\t"
      "ds_read_b128 v[108:111], v4 offset:27648\n\t"
      "v_mfma_f32_16x16x32_bf16 v[172:175], v[72:75], v[44:47], v[172:175]\n\t"
      "v_mfma_f32_16x16x32_bf16 v[176:179], v[76:79], v[32:35], v[176:179]\n\t"
      "ds_read_b128 v[112:115], v4 offset:28672\n\t"
      "v_mfma_f32_16x16x32_bf16 v[180:183], v[76:79], v[36:39], v[180:183]\n\t"
      "ds_read_b128 v[116:119], v4 offset:29696\n\t"
      "v_mfma_f32_16x16x32_bf16 v[184:187], v[76:79], v[40:43], v[184:187]\n\t"
      "v_mfma_f32_16x16x32_bf16 v[188:191], v[76:79], v[44:47], v[188:191]\n\t"
      "ds_read_b128 v[120:123], v4 offset:30720\n\t"
      "v_mfma_f32_16x16x32_bf16 v[192:195], v[80:83], v[32:35], v[192:195]\n\t"
      "ds_read_b128 v[124:127], v4 offset:31744\n\t"
      "v_mfma_f32_16x16x32_bf16 v[196:199], v[80:83], v[36:39], v[196:199]\n\t"
      "ds_add_u32 v6, v18 offset:4\n\t"
      "v_mfma_f32_16x16x32_bf16 v[200:203], v[80:83], v[40:43], v[200:203]\n\t"
      "v_add_u32 v17, s36, v3\n\t"
      "v_mfma_f32_16x16x32_bf16 v[204:207], v[80:83], v[44:47], v[204:207]\n\t"
      "v_mfma_f32_16x16x32_bf16 v[208:211], v[84:87], v[32:35], v[208:211]\n\t"
      "v_mfma_f32_16x16x32_bf16 v[212:215], v[84:87], v[36:39], v[212:215]\n\t"
      "v_mfma_f32_16x16x32_bf16 v[216:219], v[84:87], v[40:43], v[216:219]\n\t"
      "v_mfma_f32_16x16x32_bf16 v[220:223], v[84:87], v[44:47], v[220:223]\n\t"
      "v_mfma_f32_16x16x32_bf16 v[224:227], v[88:91], v[32:35], v[224:227]\n\t"
      "ds_read_b32 v20, v7 offset:0\n\t"
      "v_mfma_f32_16x16x32_bf16 v[228:231], v[88:91], v[36:39], v[228:231]\n\t"
      "v_mfma_f32_16x16x32_bf16 v[232:235], v[88:91], v[40:43], v[232:235]\n\t"
      "v_mfma_f32_16x16x32_bf16 v[236:239], v[88:91], v[44:47], v[236:239]\n\t"
      "v_mfma_f32_16x16x32_bf16 v[240:243], v[92:95], v[32:35], v[240:243]\n\t"
      "v_mfma_f32_16x16x32_bf16 v[244:247], v[92:95], v[36:39], v[244:247]\n\t"
      "s_waitcnt lgkmcnt(0)\n\t"
      "v_readfirstlane_b32 s39, v20\n\t"
      "v_mfma_f32_16x16x32_bf16 v[248:251], v[92:95], v[40:43], v[248:251]\n\t"
      "v_mfma_f32_16x16x32_bf16 v[252:255], v[92:95], v[44:47], v[252:255]\n\t"
      "s_cmp_ge_u32 s39, s37\n\t"
      "s_cbranch_scc0 Lcs2_%=\n\t"
      "Lcg2_%=:\n\t"
      "v_mfma_f32_16x16x32_bf16 v[128:131], v[96:99], v[48:51], v[128:131]\n\t"
      "ds_read_b128 v[64:67], v4 offset:0\n\t"
      "v_mfma_f32_16x16x32_bf16 v[132:135], v[96:99], v[52:55], v[132:135]\n\t"
      "ds_read_b128 v[32:35], v16 offset:0\n\t"
      "v_mfma_f32_16x16x32_bf16 v[136:139], v[96:99], v[56:59], v[136:139]\n\t"
      "v_mfma_f32_16x16x32_bf16 v[140:143], v[96:99], v[60:63], v[140:143]\n\t"
      "ds_read_b128 v[36:39], v16 offset:2048\n\t"
      "v_mfma_f32_16x16x32_bf16 v[144:147], v[100:103], v[48:51], v[144:147]\n\t"
      "ds_read_b128 v[40:43], v16 offset:4096\n\t"
      "v_mfma_f32_16x16x32_bf16 v[148:151], v[100:103], v[52:55], v[148:151]\n\t"
      "v_mfma_f32_16x16x32_bf16 v[152:155], v[100:103], v[56:59], v[152:155]\n\t"
      "ds_read_b128 v[44:47], v16 offset:6144\n\t"
      "v_mfma_f32_16x16x32_bf16 v[156:159], v[100:103], v[60:63], v[156:159]\n\t"
      "ds_read_b128 v[68:71], v4 offset:1024\n\t"
      "v_mfma_f32_16x16x32_bf16 v[160:163], v[104:107], v[48:51], v[160:163]\n\t"
      "v_mfma_f32_16x16x32_bf16 v[164:167], v[104:107], v[52:55], v[164:167]\n\t"
      "ds_read_b128 v[72:75], v4 offset:2048\n\t"
      "v_mfma_f32_16x16x32_bf16 v[168:171], v[104:107], v[56:59], v[168:171]\n\t"
      "ds_read_b128 v[76:79], v4 offset:3072\n\t"
      "v_mfma_f32_16x16x32_bf16 v[172:175], v[104:107], v[60:63], v[172:175]\n\t"
      "v_mfma_f32_16x16x32_bf16 v[176:179], v[108:111], v[48:51], v[176:179]\n\t"
      "ds_read_b128 v[80:83], v4 offset:4096\n\t"
      "v_mfma_f32_16x16x32_bf16 v[180:183], v[108:111], v[52:55], v[180:183]\n\t"
      "ds_read_b128 v[84:87], v4 offset:5120\n\t"
      "v_mfma_f32_16x16x32_bf16 v[184:187], v[108:111], v[56:59], v[184:187]\n\t"
      "v_mfma_f32_16x16x32_bf16 v[188:191], v[108:111], v[60:63], v[188:191]\n\t"
      "ds_read_b128 v[88:91], v4 offset:6144\n\t"
      "v_mfma_f32_16x16x32_bf16 v[192:195], v[112:115], v[48:51], v[192:195]\n\t"
      "ds_read_b128 v[92:95], v4 offset:7168\n\t"
      "v_mfma_f32_16x16x32_bf16 v[196:199], v[112:115], v[52:55], v[196:199]\n\t"
      "v_mfma_f32_16x16x32_bf16 v[200:203], v[112:115], v[56:59], v[200:203]\n\t"
      "v_mfma_f32_16x16x32_bf16 v[204:207], v[112:115], v[60:63], v[204:207]\n\t"
      "v_mfma_f32_16x16x32_bf16 v[208:211], v[116:119], v[48:51], v[208:211]\n\t"
      "v_mfma_f32_16x16x32_bf16 v[212:215], v[116:119], v[52:55], v[212:215]\n\t"
      "v_mfma_f32_16x16x32_bf16 v[216:219], v[116:119], v[56:59], v[216:219]\n\t"
      "v_mfma_f32_16x16x32_bf16 v[220:223], v[116:119], v[60:63], v[220:223]\n\t"
      "v_mfma_f32_16x16x32_bf16 v[224:227], v[120:123], v[48:51], v[224:227]\n\t"
      "v_mfma_f32_16x16x32_bf16 v[228:231], v[120:123], v[52:55], v[228:231]\n\t"
      "v_mfma_f32_16x16x32_bf16 v[232:235], v[120:123], v[56:59], v[232:235]\n\t"
      "v_mfma_f32_16x16x32_bf16 v[236:239], v[120:123], v[60:63], v[236:239]\n\t"
      "v_mfma_f32_16x16x32_bf16 v[240:243], v[124:127], v[48:51], v[240:243]\n\t"
      "v_mfma_f32_16x16x32_bf16 v[244:247], v[124:127], v[52:55], v[244:247]\n\t"
      "v_mfma_f32_16x16x32_bf16 v[248:251], v[124:127], v[56:59], v[248:251]\n\t"
      "v_mfma_f32_16x16x32_bf16 v[252:255], v[124:127], v[60:63], v[252:255]\n\t"
      "s_waitcnt lgkmcnt(0)\n\t"
      "s_branch Lc_loop_%=\n\t"
      "Lc_last_%=:\n\t"
      "v_mfma_f32_16x16x32_bf16 v[128:131], v[64:67], v[32:35], v[128:131]\n\t"
      "ds_read_b128 v[48:51], v17 offset:0\n\t"
      "ds_read_b128 v[96:99], v4 offset:24576\n\t"
      "v_mfma_f32_16x16x32_bf16 v[144:147], v[68:71], v[32:35], v[144:147]\n\t"
      "ds_read_b128 v[100:103], v4 offset:25600\n\t"
      "ds_read_b128 v[104:107], v4 offset:26624\n\t"
      "v_mfma_f32_16x16x32_bf16 v[160:163], v[72:75], v[32:35], v[160:163]\n\t"
      "ds_read_b128 v[108:111], v4 offset:27648\n\t"
      "ds_read_b128 v[112:115], v4 offset:28672\n\t"
      "v_mfma_f32_16x16x32_bf16 v[176:179], v[76:79], v[32:35], v[176:179]\n\t"
      "ds_read_b128 v[116:119], v4 offset:29696\n\t"
      "ds_read_b128 v[120:123], v4 offset:30720\n\t"
      "v_mfma_f32_16x16x32_bf16 v[192:195], v[80:83], v[32:35], v[192:195]\n\t"
      "ds_read_b128 v[124:127], v4 offset:31744\n\t"
      "ds_read_b128 v[52:55], v17 offset:2048\n\t"
      "v_mfma_f32_16x16x32_bf16 v[208:211], v[84:87], v[32:35], v[208:211]\n\t"
      "ds_read_b128 v[56:59], v17 offset:4096\n\t"
      "ds_read_b128 v[60:63], v17 offset:6144\n\t"
      "v_mfma_f32_16x16x32_bf16 v[224:227], v[88:91], v[32:35], v[224:227]\n\t"
      "v_mfma_f32_16x16x32_bf16 v[240:243], v[92:95], v[32:35], v[240:243]\n\t"
      "s_waitcnt lgkmcnt(9)\n\t"
      "v_mfma_f32_16x16x32_bf16 v[128:131], v[96:99], v[48:51], v[128:131]\n\t"
      "v_mfma_f32_16x16x32_bf16 v[144:147], v[100:103], v[48:51], v[144:147]\n\t"
      "s_waitcnt lgkmcnt(7)\n\t"
      "v_mfma_f32_16x16x32_bf16 v[160:163], v[104:107], v[48:51], v[160:163]\n\t"
      "v_mfma_f32_16x16x32_bf16 v[176:179], v[108:111], v[48:51], v[176:179]\n\t"
      "s_waitcnt lgkmcnt(5)\n\t"
      "v_mfma_f32_16x16x32_bf16 v[192:195], v[112:115], v[48:51], v[192:195]\n\t"
      "v_mfma_f32_16x16x32_bf16 v[208:211], v[116:119], v[48:51], v[208:211]\n\t"
      "s_waitcnt lgkmcnt(3)\n\t"
      "v_mfma_f32_16x16x32_bf16 v[224:227], v[120:123], v[48:51], v[224:227]\n\t"
      "v_mfma_f32_16x16x32_bf16 v[240:243], v[124:127], v[48:51], v[240:243]\n\t"
      "v_mfma_f32_16x16x32_bf16 v[132:135], v[64:67], v[36:39], v[132:135]\n\t"
      "v_mfma_f32_16x16x32_bf16 v[148:151], v[68:71], v[36:39], v[148:151]\n\t"
      "v_mfma_f32_16x16x32_bf16 v[164:167], v[72:75], v[36:39], v[164:167]\n\t"
      "v_mfma_f32_16x16x32_bf16 v[180:183], v[76:79], v[36:39], v[180:183]\n\t"
      "v_mfma_f32_16x16x32_bf16 v[196:199], v[80:83], v[36:39], v[196:199]\n\t"
      "v_cvt_pk_bf16_f32 v16, v128, v144\n\t"
      "v_cvt_pk_bf16_f32 v17, v160, v176\n\t"
      "v_cvt_pk_bf16_f32 v18, v129, v145\n\t"
      "v_mfma_f32_16x16x32_bf16 v[212:215], v[84:87], v[36:39], v[212:215]\n\t"
      "v_cvt_pk_bf16_f32 v19, v161, v177\n\t"
      "ds_write_b128 v11, v[16:19] offset:0\n\t"
      "v_cvt_pk_bf16_f32 v20, v130, v146\n\t"
      "v_mfma_f32_16x16x32_bf16 v[228:231], v[88:91], v[36:39], v[228:231]\n\t"
      "v_cvt_pk_bf16_f32 v21, v162, v178\n\t"
      "v_cvt_pk_bf16_f32 v22, v131, v147\n\t"
      "v_cvt_pk_bf16_f32 v23, v163, v179\n\t"
      "v_mfma_f32_16x16x32_bf16 v[244:247], v[92:95], v[36:39], v[244:247]\n\t"
      "ds_write_b128 v12, v[20:23] offset:0\n\t"
      "v_cvt_pk_bf16_f32 v24, v192, v208\n\t"
      "v_cvt_pk_bf16_f32 v25, v224, v240\n\t"
      "s_waitcnt lgkmcnt(2)\n\t"
      "v_mfma_f32_16x16x32_bf16 v[132:135], v[96:99], v[52:55], v[132:135]\n\t"
      "v_cvt_pk_bf16_f32 v26, v193, v209\n\t"
      "v_cvt_pk_bf16_f32 v27, v225, v241\n\t"
      "ds_write_b128 v11, v[24:27] offset:128\n\t"
      "v_mfma_f32_16x16x32_bf16 v[148:151], v[100:103], v[52:55], v[148:151]\n\t"
      "v_cvt_pk_bf16_f32 v28, v194, v210\n\t"
      "v_cvt_pk_bf16_f32 v29, v226, v242\n\t"
      "v_cvt_pk_bf16_f32 v30, v195, v211\n\t"
      "v_mfma_f32_16x16x32_bf16 v[164:167], v[104:107], v[52:55], v[164:167]\n\t"
      "v_cvt_pk_bf16_f32 v31, v227, v243\n\t"
      "ds_write_b128 v12, v[28:31] offset:128\n\t"
      "ds_read_b128 v[16:19], v13 offset:0\n\t"
      "v_mfma_f32_16x16x32_bf16 v[180:183], v[108:111], v[52:55], v[180:183]\n\t"
      "ds_read_b128 v[20:23], v14 offset:0\n\t"
      "ds_read_b128 v[24:27], v13 offset:2048\n\t"
      "ds_read_b128 v[28:31], v14 offset:2048\n\t"
      "v_mfma_f32_16x16x32_bf16 v[196:199], v[112:115], v[52:55], v[196:199]\n\t"
      "v_mfma_f32_16x16x32_bf16 v[212:215], v[116:119], v[52:55], v[212:215]\n\t"
      "v_mfma_f32_16x16x32_bf16 v[228:231], v[120:123], v[52:55], v[228:231]\n\t"
      "v_mfma_f32_16x16x32_bf16 v[244:247], v[124:127], v[52:55], v[244:247]\n\t"
      "v_mfma_f32_16x16x32_bf16 v[136:139], v[64:67], v[40:43], v[136:139]\n\t"
      "v_mfma_f32_16x16x32_bf16 v[152:155], v[68:71], v[40:43], v[152:155]\n\t"
      "v_mfma_f32_16x16x32_bf16 v[168:171], v[72:75], v[40:43], v[168:171]\n\t"
      "s_mov_b32 s41, 0\n\t"
      "s_waitcnt lgkmcnt(3)\n\t"
      "buffer_store_dwordx4 v[16:19], v5, s[8:11], s41 offen sc0 sc1\n\t"
      "v_mfma_f32_16x16x32_bf16 v[184:187], v[76:79], v[40:43], v[184:187]\n\t"
      "s_mov_b32 s41, 28672\n\t"
      "s_waitcnt lgkmcnt(2)\n\t"
      "buffer_store_dwordx4 v[20:23], v5, s[8:11], s41 offen sc0 sc1\n\t"
      "v_mfma_f32_16x16x32_bf16 v[200:203], v[80:83], v[40:43], v[200:203]\n\t"
      "s_mov_b32 s41, 57344\n\t"
      "s_waitcnt lgkmcnt(1)\n\t"
      "buffer_store_dwordx4 v[24:27], v5, s[8:11], s41 offen sc0 sc1\n\t"
      "v_mfma_f32_16x16x32_bf16 v[216:219], v[84:87], v[40:43], v[216:219]\n\t"
      "s_mov_b32 s41, 86016\n\t"
      "s_waitcnt lgkmcnt(0)\n\t"
      "buffer_store_dwordx4 v[28:31], v5, s[8:11], s41 offen sc0 sc1\n\t"
      "v_mfma_f32_16x16x32_bf16 v[232:235], v[88:91], v[40:43], v[232:235]\n\t"
      "v_cvt_pk_bf16_f32 v128, v132, v148\n\t"
      "v_cvt_pk_bf16_f32 v129, v164, v180\n\t"
      "v_cvt_pk_bf16_f32 v130, v133, v149\n\t"
      "v_mfma_f32_16x16x32_bf16 v[248:251], v[92:95], v[40:43], v[248:251]\n\t"
      "v_cvt_pk_bf16_f32 v131, v165, v181\n\t"
      "ds_write_b128 v11, v[128:131] offset:4096\n\t"
      "v_cvt_pk_bf16_f32 v144, v134, v150\n\t"
      "v_mfma_f32_16x16x32_bf16 v[136:139], v[96:99], v[56:59], v[136:139]\n\t"
      "v_cvt_pk_bf16_f32 v145, v166, v182\n\t"
      "v_cvt_pk_bf16_f32 v146, v135, v151\n\t"
      "v_cvt_pk_bf16_f32 v147, v167, v183\n\t"
      "v_mfma_f32_16x16x32_bf16 v[152:155], v[100:103], v[56:59], v[152:155]\n\t"
      "ds_write_b128 v12, v[144:147] offset:4096\n\t"
      "v_cvt_pk_bf16_f32 v160, v196, v212\n\t"
      "v_cvt_pk_bf16_f32 v161, v228, v244\n\t"
      "v_mfma_f32_16x16x32_bf16 v[168:171], v[104:107], v[56:59], v[168:171]\n\t"
      "v_cvt_pk_bf16_f32 v162, v197, v213\n\t"
      "v_cvt_pk_bf16_f32 v163, v229, v245\n\t"
      "ds_write_b128 v11, v[160:163] offset:4224\n\t"
      "v_mfma_f32_16x16x32_bf16 v[184:187], v[108:111], v[56:59], v[184:187]\n\t"
      "v_cvt_pk_bf16_f32 v176, v198, v214\n\t"
      "v_cvt_pk_bf16_f32 v177, v230, v246\n\t"
      "v_cvt_pk_bf16_f32 v178, v199, v215\n\t"
      "v_mfma_f32_16x16x32_bf16 v[200:203], v[112:115], v[56:59], v[200:203]\n\t"
      "v_cvt_pk_bf16_f32 v179, v231, v247\n\t"
      "ds_write_b128 v12, v[176:179] offset:4224\n\t"
      "ds_read_b128 v[128:131], v13 offset:4096\n\t"
      "v_mfma_f32_16x16x32_bf16 v[216:219], v[116:119], v[56:59], v[216:219]\n\t"
      "ds_read_b128 v[144:147], v14 offset:4096\n\t"
      "ds_read_b128 v[160:163], v13 offset:6144\n\t"
      "ds_read_b128 v[176:179], v14 offset:6144\n\t"
      "v_mfma_f32_16x16x32_bf16 v[232:235], v[120:123], v[56:59], v[232:235]\n\t"
      "v_mfma_f32_16x16x32_bf16 v[248:251], v[124:127], v[56:59], v[248:251]\n\t"
      "v_mfma_f32_16x16x32_bf16 v[140:143], v[64:67], v[44:47], v[140:143]\n\t"
      "v_mfma_f32_16x16x32_bf16 v[156:159], v[68:71], v[44:47], v[156:159]\n\t"
      "v_mfma_f32_16x16x32_bf16 v[172:175], v[72:75], v[44:47], v[172:175]\n\t"
      "s_mov_b32 s41, 114688\n\t"
      "s_waitcnt lgkmcnt(3)\n\t"
      "buffer_store_dwordx4 v[128:131], v5, s[8:11], s41 offen sc0 sc1\n\t"
      "v_mfma_f32_16x16x32_bf16 v[188:191], v[76:79], v[44:47], v[188:191]\n\t"
      "s_mov_b32 s41, 143360\n\t"
      "s_waitcnt lgkmcnt(2)\n\t"
      "buffer_store_dwordx4 v[144:147], v5, s[8:11], s41 offen sc0 sc1\n\t"
      "v_mfma_f32_16x16x32_bf16 v[204:207], v[80:83], v[44:47], v[204:207]\n\t"
      "s_mov_b32 s41, 172032\n\t"
      "s_waitcnt lgkmcnt(1)\n\t"
      "buffer_store_dwordx4 v[160:163], v5, s[8:11], s41 offen sc0 sc1\n\t"
      "v_mfma_f32_16x16x32_bf16 v[220:223], v[84:87], v[44:47], v[220:223]\n\t"
      "s_mov_b32 s41, 200704\n\t"
      "s_waitcnt lgkmcnt(0)\n\t"
      "buffer_store_dwordx4 v[176:179], v5, s[8:11], s41 offen sc0 sc1\n\t"
      "v_mfma_f32_16x16x32_bf16 v[236:239], v[88:91], v[44:47], v[236:239]\n\t"
      "v_cvt_pk_bf16_f32 v132, v136, v152\n\t"
      "v_cvt_pk_bf16_f32 v133, v168, v184\n\t"
      "v_cvt_pk_bf16_f32 v134, v137, v153\n\t"
      "v_mfma_f32_16x16x32_bf16 v[252:255], v[92:95], v[44:47], v[252:255]\n\t"
      "v_cvt_pk_bf16_f32 v135, v169, v185\n\t"
      "ds_write_b128 v11, v[132:135] offset:32768\n\t"
      "v_cvt_pk_bf16_f32 v148, v138, v154\n\t"
      "v_mfma_f32_16x16x32_bf16 v[140:143], v[96:99], v[60:63], v[140:143]\n\t"
      "v_cvt_pk_bf16_f32 v149, v170, v186\n\t"
      "v_cvt_pk_bf16_f32 v150, v139, v155\n\t"
      "v_cvt_pk_bf16_f32 v151, v171, v187\n\t"
      "v_mfma_f32_16x16x32_bf16 v[156:159], v[100:103], v[60:63], v[156:159]\n\t"
      "ds_write_b128 v12, v[148:151] offset:32768\n\t"
      "v_cvt_pk_bf16_f32 v164, v200, v216\n\t"
      "v_cvt_pk_bf16_f32 v165, v232, v248\n\t"
      "v_mfma_f32_16x16x32_bf16 v[172:175], v[104:107], v[60:63], v[172:175]\n\t"
      "v_cvt_pk_bf16_f32 v166, v201, v217\n\t"
      "v_cvt_pk_bf16_f32 v167, v233, v249\n\t"
      "ds_write_b128 v11, v[164:167] offset:32896\n\t"
      "v_mfma_f32_16x16x32_bf16 v[188:191], v[108:111], v[60:63], v[188:191]\n\t"
      "v_cvt_pk_bf16_f32 v180, v202, v218\n\t"
      "v_cvt_pk_bf16_f32 v181, v234, v250\n\t"
      "v_cvt_pk_bf16_f32 v182, v203, v219\n\t"
      "v_mfma_f32_16x16x32_bf16 v[204:207], v[112:115], v[60:63], v[204:207]\n\t"
      "v_cvt_pk_bf16_f32 v183, v235, v251\n\t"
      "ds_write_b128 v12, v[180:183] offset:32896\n\t"
      "ds_read_b128 v[132:135], v13 offset:32768\n\t"
      "v_mfma_f32_16x16x32_bf16 v[220:223], v[116:119], v[60:63], v[220:223]\n\t"
      "ds_read_b128 v[148:151], v14 offset:32768\n\t"
      "ds_read_b128 v[164:167], v13 offset:34816\n\t"
      "ds_read_b128 v[180:183], v14 offset:34816\n\t"
      "v_mfma_f32_16x16x32_bf16 v[236:239], v[120:123], v[60:63], v[236:239]\n\t"
      "v_mfma_f32_16x16x32_bf16 v[252:255], v[124:127], v[60:63], v[252:255]\n\t"
      "s_mov_b32 s41, 229376\n\t"
      "s_waitcnt lgkmcnt(3)\n\t"
      "buffer_store_dwordx4 v[132:135], v5, s[8:11], s41 offen sc0 sc1\n\t"
      "s_mov_b32 s41, 258048\n\t"
      "s_waitcnt lgkmcnt(2)\n\t"
      "buffer_store_dwordx4 v[148:151], v5, s[8:11], s41 offen sc0 sc1\n\t"
      "s_mov_b32 s41, 286720\n\t"
      "s_waitcnt lgkmcnt(1)\n\t"
      "buffer_store_dwordx4 v[164:167], v5, s[8:11], s41 offen sc0 sc1\n\t"
      "s_mov_b32 s41, 315392\n\t"
      "s_waitcnt lgkmcnt(0)\n\t"
      "buffer_store_dwordx4 v[180:183], v5, s[8:11], s41 offen sc0 sc1\n\t"
      "s_nop 7\n\t"
      "v_cvt_pk_bf16_f32 v136, v140, v156\n\t"
      "v_cvt_pk_bf16_f32 v137, v172, v188\n\t"
      "v_cvt_pk_bf16_f32 v138, v141, v157\n\t"
      "v_cvt_pk_bf16_f32 v139, v173, v189\n\t"
      "ds_write_b128 v11, v[136:139] offset:36864\n\t"
      "v_cvt_pk_bf16_f32 v152, v142, v158\n\t"
      "v_cvt_pk_bf16_f32 v153, v174, v190\n\t"
      "v_cvt_pk_bf16_f32 v154, v143, v159\n\t"
      "v_cvt_pk_bf16_f32 v155, v175, v191\n\t"
      "ds_write_b128 v12, v[152:155] offset:36864\n\t"
      "v_cvt_pk_bf16_f32 v168, v204, v220\n\t"
      "v_cvt_pk_bf16_f32 v169, v236, v252\n\t"
      "v_cvt_pk_bf16_f32 v170, v205, v221\n\t"
      "v_cvt_pk_bf16_f32 v171, v237, v253\n\t"
      "ds_write_b128 v11, v[168:171] offset:36992\n\t"
      "v_cvt_pk_bf16_f32 v184, v206, v222\n\t"
      "v_cvt_pk_bf16_f32 v185, v238, v254\n\t"
      "v_cvt_pk_bf16_f32 v186, v207, v223\n\t"
      "v_cvt_pk_bf16_f32 v187, v239, v255\n\t"
      "ds_write_b128 v12, v[184:187] offset:36992\n\t"
      "ds_read_b128 v[136:139], v13 offset:36864\n\t"
      "ds_read_b128 v[152:155], v14 offset:36864\n\t"
      "ds_read_b128 v[168:171], v13 offset:38912\n\t"
      "ds_read_b128 v[184:187], v14 offset:38912\n\t"
      "s_mov_b32 s41, 344064\n\t"
      "s_waitcnt lgkmcnt(3)\n\t"
      "buffer_store_dwordx4 v[136:139], v5, s[8:11], s41 offen sc0 sc1\n\t"
      "s_mov_b32 s41, 372736\n\t"
      "s_waitcnt lgkmcnt(2)\n\t"
      "buffer_store_dwordx4 v[152:155], v5, s[8:11], s41 offen sc0 sc1\n\t"
      "s_mov_b32 s41, 401408\n\t"
      "s_waitcnt lgkmcnt(1)\n\t"
      "buffer_store_dwordx4 v[168:171], v5, s[8:11], s41 offen sc0 sc1\n\t"
      "s_mov_b32 s41, 430080\n\t"
      "s_waitcnt lgkmcnt(0)\n\t"
      "buffer_store_dwordx4 v[184:187], v5, s[8:11], s41 offen sc0 sc1\n\t"
      "s_endpgm\n\t"
      "Lcs1_%=:\n\t"
      "ds_read_b32 v20, v7 offset:4\n\t"
      "s_add_u32 s35, s35, 1\n\t"
      "s_cmp_ge_u32 s35, 0x4000000\n\t"
      "s_cbranch_scc1 Lcg1_%=\n\t"
      "s_waitcnt lgkmcnt(0)\n\t"
      "v_readfirstlane_b32 s39, v20\n\t"
      "s_nop 1\n\t"
      "s_cmp_ge_u32 s39, s37\n\t"
      "s_cbranch_scc0 Lcs1_%=\n\t"
      "s_branch Lcg1_%=\n\t"
      "Lcs2_%=:\n\t"
      "ds_read_b32 v20, v7 offset:0\n\t"
      "s_add_u32 s35, s35, 1\n\t"
      "s_cmp_ge_u32 s35, 0x4000000\n\t"
      "s_cbranch_scc1 Lcg2_%=\n\t"
      "s_waitcnt lgkmcnt(0)\n\t"
      "v_readfirstlane_b32 s39, v20\n\t"
      "s_nop 1\n\t"
      "s_cmp_ge_u32 s39, s37\n\t"
      "s_cbranch_scc0 Lcs2_%=\n\t"
      "s_branch Lcg2_%=\n\t"
      "Lhelper_%=:\n\t"
      "s_waitcnt lgkmcnt(0)\n\t"
      "s_cmp_ge_u32 s24, 4\n\t"
      "s_cbranch_scc1 Lh_end_%=\n\t"
      "s_mov_b32 s12, s2\n\t"
      "s_and_b32 s13, s3, 0xffff\n\t"
      "s_mov_b32 s14, 14680064\n\t"
      "s_mov_b32 s15, 0x20000\n\t"
      "s_sub_u32 s27, s26, 28\n\t"
      "s_lshl_b32 s27, s27, 2\n\t"
      "s_add_u32 s27, s27, s24\n\t"
      "s_lshl_b32 s27, s27, 2\n\t"
      "s_add_u32 s27, s27, 64\n\t"
      "s_mul_i32 s40, s27, 14336\n\t"
      "s_mul_i32 s27, s25, 28\n\t"
      "s_lshr_b32 s27, s27, 3\n\t"
      "s_lshl_b32 s27, s27, 9\n\t"
      "s_add_u32 s40, s40, s27\n\t"
      "v_lshrrev_b32 v2, 4, v1\n\t"
      "v_mul_u32_u24 v2, 14336, v2\n\t"
      "v_and_b32 v3, 15, v1\n\t"
      "v_lshl_add_u32 v2, v3, 7, v2\n\t"
      "s_and_b32 s27, s25, 1\n\t"
      "s_lshl_b32 s27, s27, 1\n\t"
      "v_subrev_u32 v3, s27, v3\n\t"
      "v_cmp_gt_u32 vcc, 14, v3\n\t"
      "v_mov_b32 v3, 0x40000000\n\t"
      "s_nop 1\n\t"
      "v_cndmask_b32 v2, v3, v2, vcc\n\t"
      "s_mov_b32 s16, s0\n\t"
      "s_and_b32 s17, s1, 0xffff\n\t"
      "s_mov_b32 s18, 4194304\n\t"
      "s_mov_b32 s19, 0x20000\n\t"
      "s_sub_u32 s27, s28, 224\n\t"
      "s_lshl_b32 s27, s27, 2\n\t"
      "s_add_u32 s27, s27, s24\n\t"
      "s_lshl_b32 s42, s27, 15\n\t"
      "v_lshrrev_b32 v4, 2, v1\n\t"
      "v_lshlrev_b32 v4, 11, v4\n\t"
      "v_and_b32 v3, 3, v1\n\t"
      "v_lshl_add_u32 v4, v3, 7, v4\n\t"
      "s_waitcnt vmcnt(1)\n\t"
      "buffer_load_dword v32, v2, s[12:15], s40 offen\n\t"
      "s_add_u32 s40, s40, 917504\n\t"
      "s_waitcnt vmcnt(1)\n\t"
      "buffer_load_dword v32, v2, s[12:15], s40 offen\n\t"
      "s_add_u32 s40, s40, 917504\n\t"
      "s_waitcnt vmcnt(1)\n\t"
      "buffer_load_dword v32, v2, s[12:15], s40 offen\n\t"
      "s_add_u32 s40, s40, 917504\n\t"
      "s_waitcnt vmcnt(1)\n\t"
      "buffer_load_dword v32, v4, s[16:19], s42 offen offset:1024\n\t"
      "s_waitcnt vmcnt(1)\n\t"
      "buffer_load_dword v32, v2, s[12:15], s40 offen\n\t"
      "s_add_u32 s40, s40, 917504\n\t"
      "s_waitcnt vmcnt(1)\n\t"
      "buffer_load_dword v32, v2, s[12:15], s40 offen\n\t"
      "s_add_u32 s40, s40, 917504\n\t"
      "s_waitcnt vmcnt(1)\n\t"
      "buffer_load_dword v32, v2, s[12:15], s40 offen\n\t"
      "s_add_u32 s40, s40, 917504\n\t"
      "s_waitcnt vmcnt(1)\n\t"
      "buffer_load_dword v32, v4, s[16:19], s42 offen offset:1536\n\t"
      "s_waitcnt vmcnt(1)\n\t"
      "buffer_load_dword v32, v2, s[12:15], s40 offen\n\t"
      "s_add_u32 s40, s40, 917504\n\t"
      "s_waitcnt vmcnt(1)\n\t"
      "buffer_load_dword v32, v2, s[12:15], s40 offen\n\t"
      "s_add_u32 s40, s40, 917504\n\t"
      "s_waitcnt vmcnt(1)\n\t"
      "buffer_load_dword v32, v2, s[12:15], s40 offen\n\t"
      "s_add_u32 s40, s40, 917504\n\t"
      "s_waitcnt vmcnt(1)\n\t"
      "buffer_load_dword v32, v2, s[12:15], s40 offen\n\t"
      "s_add_u32 s40, s40, 917504\n\t"
      "s_waitcnt vmcnt(1)\n\t"
      "buffer_load_dword v32, v2, s[12:15], s40 offen\n\t"
      "s_add_u32 s40, s40, 917504\n\t"
      "s_waitcnt vmcnt(1)\n\t"
      "buffer_load_dword v32, v2, s[12:15], s40 offen\n\t"
      "s_add_u32 s40, s40, 917504\n\t"
      "s_waitcnt vmcnt(1)\n\t"
      "buffer_load_dword v32, v2, s[12:15], s40 offen\n\t"
      "s_add_u32 s40, s40, 917504\n\t"
      "s_waitcnt vmcnt(1)\n\t"
      "buffer_load_dword v32, v2, s[12:15], s40 offen\n\t"
      "s_add_u32 s40, s40, 917504\n\t"
      "s_waitcnt vmcnt(1)\n\t"
      "buffer_load_dword v32, v2, s[12:15], s40 offen\n\t"
      "s_add_u32 s40, s40, 917504\n\t"
      "Lh_end_%=:\n\t"
      "s_endpgm\n\t"
      "Lhot_end_%=:\n\t"
      "Lp_slow_%=:\n\t"
      "ds_read_b128 v[136:139], v149 offset:0\n\t"
      "ds_read_b128 v[140:143], v149 offset:512\n\t"
      "v_add_u32 v149, 2048, v149\n\t"
      "Lp_loops_%=:\n\t"
      "s_add_u32 s42, s42, 917504\n\t"
      "buffer_load_dwordx4 v[64:67], v128, s[12:15], s42 offen \n\t"
      "s_waitcnt vmcnt(13) lgkmcnt(0)\n\t"
      "v_fma_mix_f32 v112, v32, s40, -v140 op_sel_hi:[1,0,0]\n\t"
      "v_fma_mix_f32 v113, v33, s40, -v141 op_sel_hi:[1,0,0]\n\t"
      "v_fma_mix_f32 v114, v34, s40, -v142 op_sel_hi:[1,0,0]\n\t"
      "v_fma_mix_f32 v115, v35, s40, -v143 op_sel_hi:[1,0,0]\n\t"
      "v_fma_mix_f32 v116, v36, s40, -v140 op_sel_hi:[1,0,0]\n\t"
      "v_fma_mix_f32 v117, v37, s40, -v141 op_sel_hi:[1,0,0]\n\t"
      "v_fma_mix_f32 v118, v38, s40, -v142 op_sel_hi:[1,0,0]\n\t"
      "v_fma_mix_f32 v119, v39, s40, -v143 op_sel_hi:[1,0,0]\n\t"
      "buffer_load_dwordx4 v[68:71], v129, s[12:15], s42 offen \n\t"
      "v_cvt_pk_bf16_f32 v112, 0, v112\n\t"
      "v_cvt_pk_bf16_f32 v113, 0, v113\n\t"
      "v_cvt_pk_bf16_f32 v114, 0, v114\n\t"
      "v_cvt_pk_bf16_f32 v115, 0, v115\n\t"
      "v_cvt_pk_bf16_f32 v116, 0, v116\n\t"
      "v_cvt_pk_bf16_f32 v117, 0, v117\n\t"
      "v_cvt_pk_bf16_f32 v118, 0, v118\n\t"
      "v_cvt_pk_bf16_f32 v119, 0, v119\n\t"
      "buffer_load_dwordx4 v[72:75], v130, s[12:15], s42 offen \n\t"
      "v_mul_f32 v112, v112, v136\n\t"
      "v_mul_f32 v113, v113, v137\n\t"
      "v_mul_f32 v114, v114, v138\n\t"
      "v_mul_f32 v115, v115, v139\n\t"
      "v_mul_f32 v116, v116, v136\n\t"
      "v_mul_f32 v117, v117, v137\n\t"
      "v_mul_f32 v118, v118, v138\n\t"
      "v_mul_f32 v119, v119, v139\n\t"
      "v_cvt_pk_bf16_f32 v96, v112, v116\n\t"
      "v_cvt_pk_bf16_f32 v100, v113, v117\n\t"
      "v_cvt_pk_bf16_f32 v104, v114, v118\n\t"
      "v_cvt_pk_bf16_f32 v108, v115, v119\n\t"
      "v_fma_mix_f32 v112, v40, s40, -v140 op_sel_hi:[1,0,0]\n\t"
      "v_fma_mix_f32 v113, v41, s40, -v141 op_sel_hi:[1,0,0]\n\t"
      "v_fma_mix_f32 v114, v42, s40, -v142 op_sel_hi:[1,0,0]\n\t"
      "v_fma_mix_f32 v115, v43, s40, -v143 op_sel_hi:[1,0,0]\n\t"
      "v_fma_mix_f32 v116, v44, s40, -v140 op_sel_hi:[1,0,0]\n\t"
      "v_fma_mix_f32 v117, v45, s40, -v141 op_sel_hi:[1,0,0]\n\t"
      "v_fma_mix_f32 v118, v46, s40, -v142 op_sel_hi:[1,0,0]\n\t"
      "v_fma_mix_f32 v119, v47, s40, -v143 op_sel_hi:[1,0,0]\n\t"
      "buffer_load_dwordx4 v[76:79], v131, s[12:15], s42 offen \n\t"
      "v_cvt_pk_bf16_f32 v112, 0, v112\n\t"
      "v_cvt_pk_bf16_f32 v113, 0, v113\n\t"
      "v_cvt_pk_bf16_f32 v114, 0, v114\n\t"
      "v_cvt_pk_bf16_f32 v115, 0, v115\n\t"
      "v_cvt_pk_bf16_f32 v116, 0, v116\n\t"
      "v_cvt_pk_bf16_f32 v117, 0, v117\n\t"
      "v_cvt_pk_bf16_f32 v118, 0, v118\n\t"
      "v_cvt_pk_bf16_f32 v119, 0, v119\n\t"
      "buffer_load_dwordx4 v[80:83], v132, s[12:15], s42 offen \n\t"
      "v_mul_f32 v112, v112, v136\n\t"
      "v_mul_f32 v113, v113, v137\n\t"
      "v_mul_f32 v114, v114, v138\n\t"
      "v_mul_f32 v115, v115, v139\n\t"
      "v_mul_f32 v116, v116, v136\n\t"
      "v_mul_f32 v117, v117, v137\n\t"
      "v_mul_f32 v118, v118, v138\n\t"
      "v_mul_f32 v119, v119, v139\n\t"
      "v_cvt_pk_bf16_f32 v97, v112, v116\n\t"
      "v_cvt_pk_bf16_f32 v101, v113, v117\n\t"
      "v_cvt_pk_bf16_f32 v105, v114, v118\n\t"
      "v_cvt_pk_bf16_f32 v109, v115, v119\n\t"
      "s_waitcnt vmcnt(13)\n\t"
      "v_fma_mix_f32 v112, v48, s40, -v140 op_sel_hi:[1,0,0]\n\t"
      "v_fma_mix_f32 v113, v49, s40, -v141 op_sel_hi:[1,0,0]\n\t"
      "v_fma_mix_f32 v114, v50, s40, -v142 op_sel_hi:[1,0,0]\n\t"
      "v_fma_mix_f32 v115, v51, s40, -v143 op_sel_hi:[1,0,0]\n\t"
      "v_fma_mix_f32 v116, v52, s40, -v140 op_sel_hi:[1,0,0]\n\t"
      "v_fma_mix_f32 v117, v53, s40, -v141 op_sel_hi:[1,0,0]\n\t"
      "v_fma_mix_f32 v118, v54, s40, -v142 op_sel_hi:[1,0,0]\n\t"
      "v_fma_mix_f32 v119, v55, s40, -v143 op_sel_hi:[1,0,0]\n\t"
      "buffer_load_dwordx4 v[84:87], v133, s[12:15], s42 offen \n\t"
      "v_cvt_pk_bf16_f32 v112, 0, v112\n\t"
      "v_cvt_pk_bf16_f32 v113, 0, v113\n\t"
      "v_cvt_pk_bf16_f32 v114, 0, v114\n\t"
      "v_cvt_pk_bf16_f32 v115, 0, v115\n\t"
      "v_cvt_pk_bf16_f32 v116, 0, v116\n\t"
      "v_cvt_pk_bf16_f32 v117, 0, v117\n\t"
      "v_cvt_pk_bf16_f32 v118, 0, v118\n\t"
      "v_cvt_pk_bf16_f32 v119, 0, v119\n\t"
      "buffer_load_dwordx4 v[88:91], v134, s[12:15], s42 offen \n\t"
      "v_mul_f32 v112, v112, v136\n\t"
      "v_mul_f32 v113, v113, v137\n\t"
      "v_mul_f32 v114, v114, v138\n\t"
      "v_mul_f32 v115, v115, v139\n\t"
      "v_mul_f32 v116, v116, v136\n\t"
      "v_mul_f32 v117, v117, v137\n\t"
      "v_mul_f32 v118, v118, v138\n\t"
      "v_mul_f32 v119, v119, v139\n\t"
      "v_cvt_pk_bf16_f32 v98, v112, v116\n\t"
      "v_cvt_pk_bf16_f32 v102, v113, v117\n\t"
      "v_cvt_pk_bf16_f32 v106, v114, v118\n\t"
      "v_cvt_pk_bf16_f32 v110, v115, v119\n\t"
      "ds_read_b32 v144, v6 offset:8\n\t"
      "v_fma_mix_f32 v112, v56, s40, -v140 op_sel_hi:[1,0,0]\n\t"
      "v_fma_mix_f32 v113, v57, s40, -v141 op_sel_hi:[1,0,0]\n\t"
      "v_fma_mix_f32 v114, v58, s40, -v142 op_sel_hi:[1,0,0]\n\t"
      "v_fma_mix_f32 v115, v59, s40, -v143 op_sel_hi:[1,0,0]\n\t"
      "v_fma_mix_f32 v116, v60, s40, -v140 op_sel_hi:[1,0,0]\n\t"
      "v_fma_mix_f32 v117, v61, s40, -v141 op_sel_hi:[1,0,0]\n\t"
      "v_fma_mix_f32 v118, v62, s40, -v142 op_sel_hi:[1,0,0]\n\t"
      "v_fma_mix_f32 v119, v63, s40, -v143 op_sel_hi:[1,0,0]\n\t"
      "buffer_load_dwordx4 v[92:95], v135, s[12:15], s42 offen \n\t"
      "v_cvt_pk_bf16_f32 v112, 0, v112\n\t"
      "v_cvt_pk_bf16_f32 v113, 0, v113\n\t"
      "v_cvt_pk_bf16_f32 v114, 0, v114\n\t"
      "v_cvt_pk_bf16_f32 v115, 0, v115\n\t"
      "v_cvt_pk_bf16_f32 v116, 0, v116\n\t"
      "v_cvt_pk_bf16_f32 v117, 0, v117\n\t"
      "v_cvt_pk_bf16_f32 v118, 0, v118\n\t"
      "v_cvt_pk_bf16_f32 v119, 0, v119\n\t"
      "v_mul_f32 v112, v112, v136\n\t"
      "v_mul_f32 v113, v113, v137\n\t"
      "v_mul_f32 v114, v114, v138\n\t"
      "v_mul_f32 v115, v115, v139\n\t"
      "v_mul_f32 v116, v116, v136\n\t"
      "v_mul_f32 v117, v117, v137\n\t"
      "v_mul_f32 v118, v118, v138\n\t"
      "v_mul_f32 v119, v119, v139\n\t"
      "v_cvt_pk_bf16_f32 v99, v112, v116\n\t"
      "v_cvt_pk_bf16_f32 v103, v113, v117\n\t"
      "v_cvt_pk_bf16_f32 v107, v114, v118\n\t"
      "v_cvt_pk_bf16_f32 v111, v115, v119\n\t"
      "s_waitcnt lgkmcnt(0)\n\t"
      "Lpc_s0_%=:\n\t"
      "v_readfirstlane_b32 s48, v144\n\t"
      "s_nop 1\n\t"
      "s_cmp_ge_u32 s48, s47\n\t"
      "s_cbranch_scc0 Lpw_s0_%=\n\t"
      "Lpg_s0_%=:\n\t"
      "ds_write_b128 v4, v[96:99] offset:0\n\t"
      "s_add_u32 s46, s46, 32768\n\t"
      "s_cmp_lt_u32 s46, 98304\n\t"
      "ds_write_b128 v4, v[100:103] offset:1024\n\t"
      "s_cselect_b32 s46, s46, 0\n\t"
      "s_add_u32 s41, s41, 128\n\t"
      "ds_write_b128 v4, v[104:107] offset:2048\n\t"
      "s_add_u32 m0, s16, s46\n\t"
      "s_add_u32 s45, m0, 4096\n\t"
      "ds_write_b128 v4, v[108:111] offset:3072\n\t"
      "s_waitcnt vmcnt(8)\n\t"
      "ds_add_u32 v5, v148 offset:0\n\t"
      "buffer_load_dwordx4 v120, s[8:11], s41 offen offset:0  lds\n\t"
      "buffer_load_dwordx4 v121, s[8:11], s41 offen offset:1024  lds\n\t"
      "buffer_load_dwordx4 v122, s[8:11], s41 offen offset:2048  lds\n\t"
      "buffer_load_dwordx4 v123, s[8:11], s41 offen offset:3072  lds\n\t"
      "s_mov_b32 m0, s45\n\t"
      "s_nop 0\n\t"
      "buffer_load_dwordx4 v124, s[8:11], s41 offen offset:0  lds\n\t"
      "buffer_load_dwordx4 v125, s[8:11], s41 offen offset:1024  lds\n\t"
      "buffer_load_dwordx4 v126, s[8:11], s41 offen offset:2048  lds\n\t"
      "buffer_load_dwordx4 v127, s[8:11], s41 offen offset:3072  lds\n\t"
      "s_add_u32 s42, s42, 917504\n\t"
      "buffer_load_dwordx4 v[32:35], v128, s[12:15], s42 offen \n\t"
      "s_waitcnt vmcnt(13) lgkmcnt(0)\n\t"
      "v_fma_mix_f32 v112, v64, s40, -v140 op_sel_hi:[1,0,0]\n\t"
      "v_fma_mix_f32 v113, v65, s40, -v141 op_sel_hi:[1,0,0]\n\t"
      "v_fma_mix_f32 v114, v66, s40, -v142 op_sel_hi:[1,0,0]\n\t"
      "v_fma_mix_f32 v115, v67, s40, -v143 op_sel_hi:[1,0,0]\n\t"
      "v_fma_mix_f32 v116, v68, s40, -v140 op_sel_hi:[1,0,0]\n\t"
      "v_fma_mix_f32 v117, v69, s40, -v141 op_sel_hi:[1,0,0]\n\t"
      "v_fma_mix_f32 v118, v70, s40, -v142 op_sel_hi:[1,0,0]\n\t"
      "v_fma_mix_f32 v119, v71, s40, -v143 op_sel_hi:[1,0,0]\n\t"
      "buffer_load_dwordx4 v[36:39], v129, s[12:15], s42 offen \n\t"
      "v_cvt_pk_bf16_f32 v112, 0, v112\n\t"
      "v_cvt_pk_bf16_f32 v113, 0, v113\n\t"
      "v_cvt_pk_bf16_f32 v114, 0, v114\n\t"
      "v_cvt_pk_bf16_f32 v115, 0, v115\n\t"
      "v_cvt_pk_bf16_f32 v116, 0, v116\n\t"
      "v_cvt_pk_bf16_f32 v117, 0, v117\n\t"
      "v_cvt_pk_bf16_f32 v118, 0, v118\n\t"
      "v_cvt_pk_bf16_f32 v119, 0, v119\n\t"
      "buffer_load_dwordx4 v[40:43], v130, s[12:15], s42 offen \n\t"
      "v_mul_f32 v112, v112, v136\n\t"
      "v_mul_f32 v113, v113, v137\n\t"
      "v_mul_f32 v114, v114, v138\n\t"
      "v_mul_f32 v115, v115, v139\n\t"
      "v_mul_f32 v116, v116, v136\n\t"
      "v_mul_f32 v117, v117, v137\n\t"
      "v_mul_f32 v118, v118, v138\n\t"
      "v_mul_f32 v119, v119, v139\n\t"
      "v_cvt_pk_bf16_f32 v96, v112, v116\n\t"
      "v_cvt_pk_bf16_f32 v100, v113, v117\n\t"
      "v_cvt_pk_bf16_f32 v104, v114, v118\n\t"
      "v_cvt_pk_bf16_f32 v108, v115, v119\n\t"
      "v_fma_mix_f32 v112, v72, s40, -v140 op_sel_hi:[1,0,0]\n\t"
      "v_fma_mix_f32 v113, v73, s40, -v141 op_sel_hi:[1,0,0]\n\t"
      "v_fma_mix_f32 v114, v74, s40, -v142 op_sel_hi:[1,0,0]\n\t"
      "v_fma_mix_f32 v115, v75, s40, -v143 op_sel_hi:[1,0,0]\n\t"
      "v_fma_mix_f32 v116, v76, s40, -v140 op_sel_hi:[1,0,0]\n\t"
      "v_fma_mix_f32 v117, v77, s40, -v141 op_sel_hi:[1,0,0]\n\t"
      "v_fma_mix_f32 v118, v78, s40, -v142 op_sel_hi:[1,0,0]\n\t"
      "v_fma_mix_f32 v119, v79, s40, -v143 op_sel_hi:[1,0,0]\n\t"
      "buffer_load_dwordx4 v[44:47], v131, s[12:15], s42 offen \n\t"
      "v_cvt_pk_bf16_f32 v112, 0, v112\n\t"
      "v_cvt_pk_bf16_f32 v113, 0, v113\n\t"
      "v_cvt_pk_bf16_f32 v114, 0, v114\n\t"
      "v_cvt_pk_bf16_f32 v115, 0, v115\n\t"
      "v_cvt_pk_bf16_f32 v116, 0, v116\n\t"
      "v_cvt_pk_bf16_f32 v117, 0, v117\n\t"
      "v_cvt_pk_bf16_f32 v118, 0, v118\n\t"
      "v_cvt_pk_bf16_f32 v119, 0, v119\n\t"
      "buffer_load_dwordx4 v[48:51], v132, s[12:15], s42 offen \n\t"
      "v_mul_f32 v112, v112, v136\n\t"
      "v_mul_f32 v113, v113, v137\n\t"
      "v_mul_f32 v114, v114, v138\n\t"
      "v_mul_f32 v115, v115, v139\n\t"
      "v_mul_f32 v116, v116, v136\n\t"
      "v_mul_f32 v117, v117, v137\n\t"
      "v_mul_f32 v118, v118, v138\n\t"
      "v_mul_f32 v119, v119, v139\n\t"
      "v_cvt_pk_bf16_f32 v97, v112, v116\n\t"
      "v_cvt_pk_bf16_f32 v101, v113, v117\n\t"
      "v_cvt_pk_bf16_f32 v105, v114, v118\n\t"
      "v_cvt_pk_bf16_f32 v109, v115, v119\n\t"
      "s_waitcnt vmcnt(13)\n\t"
      "v_fma_mix_f32 v112, v80, s40, -v140 op_sel_hi:[1,0,0]\n\t"
      "v_fma_mix_f32 v113, v81, s40, -v141 op_sel_hi:[1,0,0]\n\t"
      "v_fma_mix_f32 v114, v82, s40, -v142 op_sel_hi:[1,0,0]\n\t"
      "v_fma_mix_f32 v115, v83, s40, -v143 op_sel_hi:[1,0,0]\n\t"
      "v_fma_mix_f32 v116, v84, s40, -v140 op_sel_hi:[1,0,0]\n\t"
      "v_fma_mix_f32 v117, v85, s40, -v141 op_sel_hi:[1,0,0]\n\t"
      "v_fma_mix_f32 v118, v86, s40, -v142 op_sel_hi:[1,0,0]\n\t"
      "v_fma_mix_f32 v119, v87, s40, -v143 op_sel_hi:[1,0,0]\n\t"
      "buffer_load_dwordx4 v[52:55], v133, s[12:15], s42 offen \n\t"
      "v_cvt_pk_bf16_f32 v112, 0, v112\n\t"
      "v_cvt_pk_bf16_f32 v113, 0, v113\n\t"
      "v_cvt_pk_bf16_f32 v114, 0, v114\n\t"
      "v_cvt_pk_bf16_f32 v115, 0, v115\n\t"
      "v_cvt_pk_bf16_f32 v116, 0, v116\n\t"
      "v_cvt_pk_bf16_f32 v117, 0, v117\n\t"
      "v_cvt_pk_bf16_f32 v118, 0, v118\n\t"
      "v_cvt_pk_bf16_f32 v119, 0, v119\n\t"
      "buffer_load_dwordx4 v[56:59], v134, s[12:15], s42 offen \n\t"
      "v_mul_f32 v112, v112, v136\n\t"
      "v_mul_f32 v113, v113, v137\n\t"
      "v_mul_f32 v114, v114, v138\n\t"
      "v_mul_f32 v115, v115, v139\n\t"
      "v_mul_f32 v116, v116, v136\n\t"
      "v_mul_f32 v117, v117, v137\n\t"
      "v_mul_f32 v118, v118, v138\n\t"
      "v_mul_f32 v119, v119, v139\n\t"
      "v_cvt_pk_bf16_f32 v98, v112, v116\n\t"
      "v_cvt_pk_bf16_f32 v102, v113, v117\n\t"
      "v_cvt_pk_bf16_f32 v106, v114, v118\n\t"
      "v_cvt_pk_bf16_f32 v110, v115, v119\n\t"
      "ds_read_b32 v144, v6 offset:12\n\t"
      "v_fma_mix_f32 v112, v88, s40, -v140 op_sel_hi:[1,0,0]\n\t"
      "v_fma_mix_f32 v113, v89, s40, -v141 op_sel_hi:[1,0,0]\n\t"
      "v_fma_mix_f32 v114, v90, s40, -v142 op_sel_hi:[1,0,0]\n\t"
      "v_fma_mix_f32 v115, v91, s40, -v143 op_sel_hi:[1,0,0]\n\t"
      "v_fma_mix_f32 v116, v92, s40, -v140 op_sel_hi:[1,0,0]\n\t"
      "v_fma_mix_f32 v117, v93, s40, -v141 op_sel_hi:[1,0,0]\n\t"
      "v_fma_mix_f32 v118, v94, s40, -v142 op_sel_hi:[1,0,0]\n\t"
      "v_fma_mix_f32 v119, v95, s40, -v143 op_sel_hi:[1,0,0]\n\t"
      "buffer_load_dwordx4 v[60:63], v135, s[12:15], s42 offen \n\t"
      "v_cvt_pk_bf16_f32 v112, 0, v112\n\t"
      "v_cvt_pk_bf16_f32 v113, 0, v113\n\t"
      "v_cvt_pk_bf16_f32 v114, 0, v114\n\t"
      "v_cvt_pk_bf16_f32 v115, 0, v115\n\t"
      "v_cvt_pk_bf16_f32 v116, 0, v116\n\t"
      "v_cvt_pk_bf16_f32 v117, 0, v117\n\t"
      "v_cvt_pk_bf16_f32 v118, 0, v118\n\t"
      "v_cvt_pk_bf16_f32 v119, 0, v119\n\t"
      "v_mul_f32 v112, v112, v136\n\t"
      "v_mul_f32 v113, v113, v137\n\t"
      "v_mul_f32 v114, v114, v138\n\t"
      "v_mul_f32 v115, v115, v139\n\t"
      "v_mul_f32 v116, v116, v136\n\t"
      "v_mul_f32 v117, v117, v137\n\t"
      "v_mul_f32 v118, v118, v138\n\t"
      "v_mul_f32 v119, v119, v139\n\t"
      "v_cvt_pk_bf16_f32 v99, v112, v116\n\t"
      "v_cvt_pk_bf16_f32 v103, v113, v117\n\t"
      "v_cvt_pk_bf16_f32 v107, v114, v118\n\t"
      "v_cvt_pk_bf16_f32 v111, v115, v119\n\t"
      "s_waitcnt lgkmcnt(0)\n\t"
      "Lpc_s1_%=:\n\t"
      "v_readfirstlane_b32 s48, v144\n\t"
      "s_nop 1\n\t"
      "s_cmp_ge_u32 s48, s47\n\t"
      "s_cbranch_scc0 Lpw_s1_%=\n\t"
      "Lpg_s1_%=:\n\t"
      "ds_read_b128 v[136:139], v149 offset:0\n\t"
      "ds_read_b128 v[140:143], v149 offset:512\n\t"
      "v_add_u32 v149, 2048, v149\n\t"
      "ds_write_b128 v4, v[96:99] offset:16384\n\t"
      "s_add_u32 s46, s46, 32768\n\t"
      "s_cmp_lt_u32 s46, 98304\n\t"
      "ds_write_b128 v4, v[100:103] offset:17408\n\t"
      "s_cselect_b32 s46, s46, 0\n\t"
      "s_add_u32 s41, s41, 128\n\t"
      "ds_write_b128 v4, v[104:107] offset:18432\n\t"
      "s_add_u32 m0, s16, s46\n\t"
      "s_add_u32 s45, m0, 4096\n\t"
      "ds_write_b128 v4, v[108:111] offset:19456\n\t"
      "s_waitcnt vmcnt(8)\n\t"
      "ds_add_u32 v5, v148 offset:4\n\t"
      "buffer_load_dwordx4 v120, s[8:11], s41 offen offset:0  lds\n\t"
      "buffer_load_dwordx4 v121, s[8:11], s41 offen offset:1024  lds\n\t"
      "buffer_load_dwordx4 v122, s[8:11], s41 offen offset:2048  lds\n\t"
      "buffer_load_dwordx4 v123, s[8:11], s41 offen offset:3072  lds\n\t"
      "s_mov_b32 m0, s45\n\t"
      "s_nop 0\n\t"
      "buffer_load_dwordx4 v124, s[8:11], s41 offen offset:0  lds\n\t"
      "buffer_load_dwordx4 v125, s[8:11], s41 offen offset:1024  lds\n\t"
      "buffer_load_dwordx4 v126, s[8:11], s41 offen offset:2048  lds\n\t"
      "buffer_load_dwordx4 v127, s[8:11], s41 offen offset:3072  lds\n\t"
      "s_add_u32 s47, s47, 4\n\t"
      "s_add_u32 s44, s44, 2\n\t"
      "s_cmp_lt_u32 s44, 14\n\t"
      "s_cbranch_scc1 Lp_loops_%=\n\t"
      "s_add_u32 s42, s42, 917504\n\t"
      "buffer_load_dwordx4 v[64:67], v128, s[12:15], s42 offen \n\t"
      "s_waitcnt vmcnt(13) lgkmcnt(0)\n\t"
      "v_fma_mix_f32 v112, v32, s40, -v140 op_sel_hi:[1,0,0]\n\t"
      "v_fma_mix_f32 v113, v33, s40, -v141 op_sel_hi:[1,0,0]\n\t"
      "v_fma_mix_f32 v114, v34, s40, -v142 op_sel_hi:[1,0,0]\n\t"
      "v_fma_mix_f32 v115, v35, s40, -v143 op_sel_hi:[1,0,0]\n\t"
      "v_fma_mix_f32 v116, v36, s40, -v140 op_sel_hi:[1,0,0]\n\t"
      "v_fma_mix_f32 v117, v37, s40, -v141 op_sel_hi:[1,0,0]\n\t"
      "v_fma_mix_f32 v118, v38, s40, -v142 op_sel_hi:[1,0,0]\n\t"
      "v_fma_mix_f32 v119, v39, s40, -v143 op_sel_hi:[1,0,0]\n\t"
      "buffer_load_dwordx4 v[68:71], v129, s[12:15], s42 offen \n\t"
      "v_cvt_pk_bf16_f32 v112, 0, v112\n\t"
      "v_cvt_pk_bf16_f32 v113, 0, v113\n\t"
      "v_cvt_pk_bf16_f32 v114, 0, v114\n\t"
      "v_cvt_pk_bf16_f32 v115, 0, v115\n\t"
      "v_cvt_pk_bf16_f32 v116, 0, v116\n\t"
      "v_cvt_pk_bf16_f32 v117, 0, v117\n\t"
      "v_cvt_pk_bf16_f32 v118, 0, v118\n\t"
      "v_cvt_pk_bf16_f32 v119, 0, v119\n\t"
      "buffer_load_dwordx4 v[72:75], v130, s[12:15], s42 offen \n\t"
      "v_mul_f32 v112, v112, v136\n\t"
      "v_mul_f32 v113, v113, v137\n\t"
      "v_mul_f32 v114, v114, v138\n\t"
      "v_mul_f32 v115, v115, v139\n\t"
      "v_mul_f32 v116, v116, v136\n\t"
      "v_mul_f32 v117, v117, v137\n\t"
      "v_mul_f32 v118, v118, v138\n\t"
      "v_mul_f32 v119, v119, v139\n\t"
      "v_cvt_pk_bf16_f32 v96, v112, v116\n\t"
      "v_cvt_pk_bf16_f32 v100, v113, v117\n\t"
      "v_cvt_pk_bf16_f32 v104, v114, v118\n\t"
      "v_cvt_pk_bf16_f32 v108, v115, v119\n\t"
      "v_fma_mix_f32 v112, v40, s40, -v140 op_sel_hi:[1,0,0]\n\t"
      "v_fma_mix_f32 v113, v41, s40, -v141 op_sel_hi:[1,0,0]\n\t"
      "v_fma_mix_f32 v114, v42, s40, -v142 op_sel_hi:[1,0,0]\n\t"
      "v_fma_mix_f32 v115, v43, s40, -v143 op_sel_hi:[1,0,0]\n\t"
      "v_fma_mix_f32 v116, v44, s40, -v140 op_sel_hi:[1,0,0]\n\t"
      "v_fma_mix_f32 v117, v45, s40, -v141 op_sel_hi:[1,0,0]\n\t"
      "v_fma_mix_f32 v118, v46, s40, -v142 op_sel_hi:[1,0,0]\n\t"
      "v_fma_mix_f32 v119, v47, s40, -v143 op_sel_hi:[1,0,0]\n\t"
      "buffer_load_dwordx4 v[76:79], v131, s[12:15], s42 offen \n\t"
      "v_cvt_pk_bf16_f32 v112, 0, v112\n\t"
      "v_cvt_pk_bf16_f32 v113, 0, v113\n\t"
      "v_cvt_pk_bf16_f32 v114, 0, v114\n\t"
      "v_cvt_pk_bf16_f32 v115, 0, v115\n\t"
      "v_cvt_pk_bf16_f32 v116, 0, v116\n\t"
      "v_cvt_pk_bf16_f32 v117, 0, v117\n\t"
      "v_cvt_pk_bf16_f32 v118, 0, v118\n\t"
      "v_cvt_pk_bf16_f32 v119, 0, v119\n\t"
      "buffer_load_dwordx4 v[80:83], v132, s[12:15], s42 offen \n\t"
      "v_mul_f32 v112, v112, v136\n\t"
      "v_mul_f32 v113, v113, v137\n\t"
      "v_mul_f32 v114, v114, v138\n\t"
      "v_mul_f32 v115, v115, v139\n\t"
      "v_mul_f32 v116, v116, v136\n\t"
      "v_mul_f32 v117, v117, v137\n\t"
      "v_mul_f32 v118, v118, v138\n\t"
      "v_mul_f32 v119, v119, v139\n\t"
      "v_cvt_pk_bf16_f32 v97, v112, v116\n\t"
      "v_cvt_pk_bf16_f32 v101, v113, v117\n\t"
      "v_cvt_pk_bf16_f32 v105, v114, v118\n\t"
      "v_cvt_pk_bf16_f32 v109, v115, v119\n\t"
      "s_waitcnt vmcnt(13)\n\t"
      "v_fma_mix_f32 v112, v48, s40, -v140 op_sel_hi:[1,0,0]\n\t"
      "v_fma_mix_f32 v113, v49, s40, -v141 op_sel_hi:[1,0,0]\n\t"
      "v_fma_mix_f32 v114, v50, s40, -v142 op_sel_hi:[1,0,0]\n\t"
      "v_fma_mix_f32 v115, v51, s40, -v143 op_sel_hi:[1,0,0]\n\t"
      "v_fma_mix_f32 v116, v52, s40, -v140 op_sel_hi:[1,0,0]\n\t"
      "v_fma_mix_f32 v117, v53, s40, -v141 op_sel_hi:[1,0,0]\n\t"
      "v_fma_mix_f32 v118, v54, s40, -v142 op_sel_hi:[1,0,0]\n\t"
      "v_fma_mix_f32 v119, v55, s40, -v143 op_sel_hi:[1,0,0]\n\t"
      "buffer_load_dwordx4 v[84:87], v133, s[12:15], s42 offen \n\t"
      "v_cvt_pk_bf16_f32 v112, 0, v112\n\t"
      "v_cvt_pk_bf16_f32 v113, 0, v113\n\t"
      "v_cvt_pk_bf16_f32 v114, 0, v114\n\t"
      "v_cvt_pk_bf16_f32 v115, 0, v115\n\t"
      "v_cvt_pk_bf16_f32 v116, 0, v116\n\t"
      "v_cvt_pk_bf16_f32 v117, 0, v117\n\t"
      "v_cvt_pk_bf16_f32 v118, 0, v118\n\t"
      "v_cvt_pk_bf16_f32 v119, 0, v119\n\t"
      "buffer_load_dwordx4 v[88:91], v134, s[12:15], s42 offen \n\t"
      "v_mul_f32 v112, v112, v136\n\t"
      "v_mul_f32 v113, v113, v137\n\t"
      "v_mul_f32 v114, v114, v138\n\t"
      "v_mul_f32 v115, v115, v139\n\t"
      "v_mul_f32 v116, v116, v136\n\t"
      "v_mul_f32 v117, v117, v137\n\t"
      "v_mul_f32 v118, v118, v138\n\t"
      "v_mul_f32 v119, v119, v139\n\t"
      "v_cvt_pk_bf16_f32 v98, v112, v116\n\t"
      "v_cvt_pk_bf16_f32 v102, v113, v117\n\t"
      "v_cvt_pk_bf16_f32 v106, v114, v118\n\t"
      "v_cvt_pk_bf16_f32 v110, v115, v119\n\t"
      "ds_read_b32 v144, v6 offset:8\n\t"
      "v_fma_mix_f32 v112, v56, s40, -v140 op_sel_hi:[1,0,0]\n\t"
      "v_fma_mix_f32 v113, v57, s40, -v141 op_sel_hi:[1,0,0]\n\t"
      "v_fma_mix_f32 v114, v58, s40, -v142 op_sel_hi:[1,0,0]\n\t"
      "v_fma_mix_f32 v115, v59, s40, -v143 op_sel_hi:[1,0,0]\n\t"
      "v_fma_mix_f32 v116, v60, s40, -v140 op_sel_hi:[1,0,0]\n\t"
      "v_fma_mix_f32 v117, v61, s40, -v141 op_sel_hi:[1,0,0]\n\t"
      "v_fma_mix_f32 v118, v62, s40, -v142 op_sel_hi:[1,0,0]\n\t"
      "v_fma_mix_f32 v119, v63, s40, -v143 op_sel_hi:[1,0,0]\n\t"
      "buffer_load_dwordx4 v[92:95], v135, s[12:15], s42 offen \n\t"
      "v_cvt_pk_bf16_f32 v112, 0, v112\n\t"
      "v_cvt_pk_bf16_f32 v113, 0, v113\n\t"
      "v_cvt_pk_bf16_f32 v114, 0, v114\n\t"
      "v_cvt_pk_bf16_f32 v115, 0, v115\n\t"
      "v_cvt_pk_bf16_f32 v116, 0, v116\n\t"
      "v_cvt_pk_bf16_f32 v117, 0, v117\n\t"
      "v_cvt_pk_bf16_f32 v118, 0, v118\n\t"
      "v_cvt_pk_bf16_f32 v119, 0, v119\n\t"
      "v_mul_f32 v112, v112, v136\n\t"
      "v_mul_f32 v113, v113, v137\n\t"
      "v_mul_f32 v114, v114, v138\n\t"
      "v_mul_f32 v115, v115, v139\n\t"
      "v_mul_f32 v116, v116, v136\n\t"
      "v_mul_f32 v117, v117, v137\n\t"
      "v_mul_f32 v118, v118, v138\n\t"
      "v_mul_f32 v119, v119, v139\n\t"
      "v_cvt_pk_bf16_f32 v99, v112, v116\n\t"
      "v_cvt_pk_bf16_f32 v103, v113, v117\n\t"
      "v_cvt_pk_bf16_f32 v107, v114, v118\n\t"
      "v_cvt_pk_bf16_f32 v111, v115, v119\n\t"
      "s_waitcnt lgkmcnt(0)\n\t"
      "Lpc_st0_%=:\n\t"
      "v_readfirstlane_b32 s48, v144\n\t"
      "s_nop 1\n\t"
      "s_cmp_ge_u32 s48, s47\n\t"
      "s_cbranch_scc0 Lpw_st0_%=\n\t"
      "Lpg_st0_%=:\n\t"
      "ds_write_b128 v4, v[96:99] offset:0\n\t"
      "s_add_u32 s46, s46, 32768\n\t"
      "s_cmp_lt_u32 s46, 98304\n\t"
      "ds_write_b128 v4, v[100:103] offset:1024\n\t"
      "s_cselect_b32 s46, s46, 0\n\t"
      "s_add_u32 s41, s41, 128\n\t"
      "ds_write_b128 v4, v[104:107] offset:2048\n\t"
      "s_add_u32 m0, s16, s46\n\t"
      "s_add_u32 s45, m0, 4096\n\t"
      "ds_write_b128 v4, v[108:111] offset:3072\n\t"
      "s_waitcnt vmcnt(8)\n\t"
      "ds_add_u32 v5, v148 offset:0\n\t"
      "buffer_load_dwordx4 v120, s[8:11], s41 offen offset:0  lds\n\t"
      "buffer_load_dwordx4 v121, s[8:11], s41 offen offset:1024  lds\n\t"
      "buffer_load_dwordx4 v122, s[8:11], s41 offen offset:2048  lds\n\t"
      "buffer_load_dwordx4 v123, s[8:11], s41 offen offset:3072  lds\n\t"
      "s_mov_b32 m0, s45\n\t"
      "s_nop 0\n\t"
      "buffer_load_dwordx4 v124, s[8:11], s41 offen offset:0  lds\n\t"
      "buffer_load_dwordx4 v125, s[8:11], s41 offen offset:1024  lds\n\t"
      "buffer_load_dwordx4 v126, s[8:11], s41 offen offset:2048  lds\n\t"
      "buffer_load_dwordx4 v127, s[8:11], s41 offen offset:3072  lds\n\t"
      "s_waitcnt vmcnt(12) lgkmcnt(0)\n\t"
      "v_fma_mix_f32 v112, v64, s40, -v140 op_sel_hi:[1,0,0]\n\t"
      "v_fma_mix_f32 v113, v65, s40, -v141 op_sel_hi:[1,0,0]\n\t"
      "v_fma_mix_f32 v114, v66, s40, -v142 op_sel_hi:[1,0,0]\n\t"
      "v_fma_mix_f32 v115, v67, s40, -v143 op_sel_hi:[1,0,0]\n\t"
      "v_fma_mix_f32 v116, v68, s40, -v140 op_sel_hi:[1,0,0]\n\t"
      "v_fma_mix_f32 v117, v69, s40, -v141 op_sel_hi:[1,0,0]\n\t"
      "v_fma_mix_f32 v118, v70, s40, -v142 op_sel_hi:[1,0,0]\n\t"
      "v_fma_mix_f32 v119, v71, s40, -v143 op_sel_hi:[1,0,0]\n\t"
      "v_cvt_pk_bf16_f32 v112, 0, v112\n\t"
      "v_cvt_pk_bf16_f32 v113, 0, v113\n\t"
      "v_cvt_pk_bf16_f32 v114, 0, v114\n\t"
      "v_cvt_pk_bf16_f32 v115, 0, v115\n\t"
      "v_cvt_pk_bf16_f32 v116, 0, v116\n\t"
      "v_cvt_pk_bf16_f32 v117, 0, v117\n\t"
      "v_cvt_pk_bf16_f32 v118, 0, v118\n\t"
      "v_cvt_pk_bf16_f32 v119, 0, v119\n\t"
      "v_mul_f32 v112, v112, v136\n\t"
      "v_mul_f32 v113, v113, v137\n\t"
      "v_mul_f32 v114, v114, v138\n\t"
      "v_mul_f32 v115, v115, v139\n\t"
      "v_mul_f32 v116, v116, v136\n\t"
      "v_mul_f32 v117, v117, v137\n\t"
      "v_mul_f32 v118, v118, v138\n\t"
      "v_mul_f32 v119, v119, v139\n\t"
      "v_cvt_pk_bf16_f32 v96, v112, v116\n\t"
      "v_cvt_pk_bf16_f32 v100, v113, v117\n\t"
      "v_cvt_pk_bf16_f32 v104, v114, v118\n\t"
      "v_cvt_pk_bf16_f32 v108, v115, v119\n\t"
      "v_fma_mix_f32 v112, v72, s40, -v140 op_sel_hi:[1,0,0]\n\t"
      "v_fma_mix_f32 v113, v73, s40, -v141 op_sel_hi:[1,0,0]\n\t"
      "v_fma_mix_f32 v114, v74, s40, -v142 op_sel_hi:[1,0,0]\n\t"
      "v_fma_mix_f32 v115, v75, s40, -v143 op_sel_hi:[1,0,0]\n\t"
      "v_fma_mix_f32 v116, v76, s40, -v140 op_sel_hi:[1,0,0]\n\t"
      "v_fma_mix_f32 v117, v77, s40, -v141 op_sel_hi:[1,0,0]\n\t"
      "v_fma_mix_f32 v118, v78, s40, -v142 op_sel_hi:[1,0,0]\n\t"
      "v_fma_mix_f32 v119, v79, s40, -v143 op_sel_hi:[1,0,0]\n\t"
      "v_cvt_pk_bf16_f32 v112, 0, v112\n\t"
      "v_cvt_pk_bf16_f32 v113, 0, v113\n\t"
      "v_cvt_pk_bf16_f32 v114, 0, v114\n\t"
      "v_cvt_pk_bf16_f32 v115, 0, v115\n\t"
      "v_cvt_pk_bf16_f32 v116, 0, v116\n\t"
      "v_cvt_pk_bf16_f32 v117, 0, v117\n\t"
      "v_cvt_pk_bf16_f32 v118, 0, v118\n\t"
      "v_cvt_pk_bf16_f32 v119, 0, v119\n\t"
      "v_mul_f32 v112, v112, v136\n\t"
      "v_mul_f32 v113, v113, v137\n\t"
      "v_mul_f32 v114, v114, v138\n\t"
      "v_mul_f32 v115, v115, v139\n\t"
      "v_mul_f32 v116, v116, v136\n\t"
      "v_mul_f32 v117, v117, v137\n\t"
      "v_mul_f32 v118, v118, v138\n\t"
      "v_mul_f32 v119, v119, v139\n\t"
      "v_cvt_pk_bf16_f32 v97, v112, v116\n\t"
      "v_cvt_pk_bf16_f32 v101, v113, v117\n\t"
      "v_cvt_pk_bf16_f32 v105, v114, v118\n\t"
      "v_cvt_pk_bf16_f32 v109, v115, v119\n\t"
      "s_waitcnt vmcnt(8)\n\t"
      "v_fma_mix_f32 v112, v80, s40, -v140 op_sel_hi:[1,0,0]\n\t"
      "v_fma_mix_f32 v113, v81, s40, -v141 op_sel_hi:[1,0,0]\n\t"
      "v_fma_mix_f32 v114, v82, s40, -v142 op_sel_hi:[1,0,0]\n\t"
      "v_fma_mix_f32 v115, v83, s40, -v143 op_sel_hi:[1,0,0]\n\t"
      "v_fma_mix_f32 v116, v84, s40, -v140 op_sel_hi:[1,0,0]\n\t"
      "v_fma_mix_f32 v117, v85, s40, -v141 op_sel_hi:[1,0,0]\n\t"
      "v_fma_mix_f32 v118, v86, s40, -v142 op_sel_hi:[1,0,0]\n\t"
      "v_fma_mix_f32 v119, v87, s40, -v143 op_sel_hi:[1,0,0]\n\t"
      "v_cvt_pk_bf16_f32 v112, 0, v112\n\t"
      "v_cvt_pk_bf16_f32 v113, 0, v113\n\t"
      "v_cvt_pk_bf16_f32 v114, 0, v114\n\t"
      "v_cvt_pk_bf16_f32 v115, 0, v115\n\t"
      "v_cvt_pk_bf16_f32 v116, 0, v116\n\t"
      "v_cvt_pk_bf16_f32 v117, 0, v117\n\t"
      "v_cvt_pk_bf16_f32 v118, 0, v118\n\t"
      "v_cvt_pk_bf16_f32 v119, 0, v119\n\t"
      "v_mul_f32 v112, v112, v136\n\t"
      "v_mul_f32 v113, v113, v137\n\t"
      "v_mul_f32 v114, v114, v138\n\t"
      "v_mul_f32 v115, v115, v139\n\t"
      "v_mul_f32 v116, v116, v136\n\t"
      "v_mul_f32 v117, v117, v137\n\t"
      "v_mul_f32 v118, v118, v138\n\t"
      "v_mul_f32 v119, v119, v139\n\t"
      "v_cvt_pk_bf16_f32 v98, v112, v116\n\t"
      "v_cvt_pk_bf16_f32 v102, v113, v117\n\t"
      "v_cvt_pk_bf16_f32 v106, v114, v118\n\t"
      "v_cvt_pk_bf16_f32 v110, v115, v119\n\t"
      "ds_read_b32 v144, v6 offset:12\n\t"
      "v_fma_mix_f32 v112, v88, s40, -v140 op_sel_hi:[1,0,0]\n\t"
      "v_fma_mix_f32 v113, v89, s40, -v141 op_sel_hi:[1,0,0]\n\t"
      "v_fma_mix_f32 v114, v90, s40, -v142 op_sel_hi:[1,0,0]\n\t"
      "v_fma_mix_f32 v115, v91, s40, -v143 op_sel_hi:[1,0,0]\n\t"
      "v_fma_mix_f32 v116, v92, s40, -v140 op_sel_hi:[1,0,0]\n\t"
      "v_fma_mix_f32 v117, v93, s40, -v141 op_sel_hi:[1,0,0]\n\t"
      "v_fma_mix_f32 v118, v94, s40, -v142 op_sel_hi:[1,0,0]\n\t"
      "v_fma_mix_f32 v119, v95, s40, -v143 op_sel_hi:[1,0,0]\n\t"
      "v_cvt_pk_bf16_f32 v112, 0, v112\n\t"
      "v_cvt_pk_bf16_f32 v113, 0, v113\n\t"
      "v_cvt_pk_bf16_f32 v114, 0, v114\n\t"
      "v_cvt_pk_bf16_f32 v115, 0, v115\n\t"
      "v_cvt_pk_bf16_f32 v116, 0, v116\n\t"
      "v_cvt_pk_bf16_f32 v117, 0, v117\n\t"
      "v_cvt_pk_bf16_f32 v118, 0, v118\n\t"
      "v_cvt_pk_bf16_f32 v119, 0, v119\n\t"
      "v_mul_f32 v112, v112, v136\n\t"
      "v_mul_f32 v113, v113, v137\n\t"
      "v_mul_f32 v114, v114, v138\n\t"
      "v_mul_f32 v115, v115, v139\n\t"
      "v_mul_f32 v116, v116, v136\n\t"
      "v_mul_f32 v117, v117, v137\n\t"
      "v_mul_f32 v118, v118, v138\n\t"
      "v_mul_f32 v119, v119, v139\n\t"
      "v_cvt_pk_bf16_f32 v99, v112, v116\n\t"
      "v_cvt_pk_bf16_f32 v103, v113, v117\n\t"
      "v_cvt_pk_bf16_f32 v107, v114, v118\n\t"
      "v_cvt_pk_bf16_f32 v111, v115, v119\n\t"
      "s_waitcnt lgkmcnt(0)\n\t"
      "Lpc_st1_%=:\n\t"
      "v_readfirstlane_b32 s48, v144\n\t"
      "s_nop 1\n\t"
      "s_cmp_ge_u32 s48, s47\n\t"
      "s_cbranch_scc0 Lpw_st1_%=\n\t"
      "Lpg_st1_%=:\n\t"
      "ds_write_b128 v4, v[96:99] offset:16384\n\t"
      "ds_write_b128 v4, v[100:103] offset:17408\n\t"
      "ds_write_b128 v4, v[104:107] offset:18432\n\t"
      "ds_write_b128 v4, v[108:111] offset:19456\n\t"
      "s_waitcnt vmcnt(0)\n\t"
      "ds_add_u32 v5, v148 offset:4\n\t"
      "s_add_u32 s47, s47, 4\n\t"
      "s_branch Lp_end_%=\n\t"
      "Lpw_s0_%=:\n\t"
      "ds_read_b32 v144, v6 offset:8\n\t"
      "s_add_u32 s39, s39, 1\n\t"
      "s_cmp_lt_u32 s39, 0x4000000\n\t"
      "s_waitcnt lgkmcnt(0)\n\t"
      "s_cbranch_scc1 Lpc_s0_%=\n\t"
      "s_branch Lpg_s0_%=\n\t"
      "Lpw_s1_%=:\n\t"
      "ds_read_b32 v144, v6 offset:12\n\t"
      "s_add_u32 s39, s39, 1\n\t"
      "s_cmp_lt_u32 s39, 0x4000000\n\t"
      "s_waitcnt lgkmcnt(0)\n\t"
      "s_cbranch_scc1 Lpc_s1_%=\n\t"
      "s_branch Lpg_s1_%=\n\t"
      "Lpw_st0_%=:\n\t"
      "ds_read_b32 v144, v6 offset:8\n\t"
      "s_add_u32 s39, s39, 1\n\t"
      "s_cmp_lt_u32 s39, 0x4000000\n\t"
      "s_waitcnt lgkmcnt(0)\n\t"
      "s_cbranch_scc1 Lpc_st0_%=\n\t"
      "s_branch Lpg_st0_%=\n\t"
      "Lpw_st1_%=:\n\t"
      "ds_read_b32 v144, v6 offset:12\n\t"
      "s_add_u32 s39, s39, 1\n\t"
      "s_cmp_lt_u32 s39, 0x4000000\n\t"
      "s_waitcnt lgkmcnt(0)\n\t"
      "s_cbranch_scc1 Lpc_st1_%=\n\t"
      "s_branch Lpg_st1_%=\n\t"
      :
      : "s"(__builtin_amdgcn_kernarg_segment_ptr()), "s"(blockIdx.x), "v"(threadIdx.x), "s"((unsigned)(size_t)lds)
      : "v1", "v2", "v3", "v4", "v5", "v6", "v7", "v8", "v9", "v10", "v11", "v12", "v13", "v14", "v15", "v16", "v17", "v18", "v19", "v20", "v21", "v22", "v23", "v24", "v25", "v26", "v27", "v28", "v29", "v30", "v31", "v32", "v33", "v34", "v35", "v36", "v37", "v38", "v39", "v40", "v41", "v42", "v43", "v44", "v45", "v46", "v47", "v48", "v49", "v50", "v51", "v52", "v53", "v54", "v55", "v56", "v57", "v58", "v59", "v60", "v61", "v62", "v63", "v64", "v65", "v66", "v67", "v68", "v69", "v70", "v71", "v72", "v73", "v74", "v75", "v76", "v77", "v78", "v79", "v80", "v81", "v82", "v83", "v84", "v85", "v86", "v87", "v88", "v89", "v90", "v91", "v92", "v93", "v94", "v95", "v96", "v97", "v98", "v99", "v100", "v101", "v102", "v103", "v104", "v105", "v106", "v107", "v108", "v109", "v110", "v111", "v112", "v113", "v114", "v115", "v116", "v117", "v118", "v119", "v120", "v121", "v122", "v123", "v124", "v125", "v126", "v127", "v128", "v129", "v130", "v131", "v132", "v133", "v134", "v135", "v136", "v137", "v138", "v139", "v140", "v141", "v142", "v143", "v144", "v145", "v146", "v147", "v148", "v149", "v150", "v151", "v152", "v153", "v154", "v155", "v156", "v157", "v158", "v159", "v160", "v161", "v162", "v163", "v164", "v165", "v166", "v167", "v168", "v169", "v170", "v171", "v172", "v173", "v174", "v175", "v176", "v177", "v178", "v179", "v180", "v181", "v182", "v183", "v184", "v185", "v186", "v187", "v188", "v189", "v190", "v191", "v192", "v193", "v194", "v195", "v196", "v197", "v198", "v199", "v200", "v201", "v202", "v203", "v204", "v205", "v206", "v207", "v208", "v209", "v210", "v211", "v212", "v213", "v214", "v215", "v216", "v217", "v218", "v219", "v220", "v221", "v222", "v223", "v224", "v225", "v226", "v227", "v228", "v229", "v230", "v231", "v232", "v233", "v234", "v235", "v236", "v237", "v238", "v239", "v240", "v241", "v242", "v243", "v244", "v245", "v246", "v247", "v248", "v249", "v250", "v251", "v252", "v253", "v254", "v255", "s0", "s1", "s2", "s3", "s4", "s5", "s6", "s7", "s8", "s9", "s10", "s11", "s12", "s13", "s14", "s15", "s16", "s17", "s18", "s19", "s20", "s21", "s22", "s23", "s24", "s25", "s26", "s27", "s28", "s29", "s30", "s31", "s32", "s33", "s34", "s35", "s36", "s37", "s38", "s39", "s40", "s41", "s42", "s43", "s44", "s45", "s46", "s47", "s48", "s49", "s50", "s51", "s52", "s53", "m0", "vcc", "scc", "memory");
}

extern "C" void kernel_launch(void* const* d_in, const int* in_sizes, int n_in, void* d_out, int out_size, void* d_ws,
                              size_t ws_size, hipStream_t stream) {
    w4a16_fused<<<256, 512, 0, stream>>>((const u16*)d_in[0], (const int*)d_in[1], (const u16*)d_in[2],
                                               (const u16*)d_in[3], (u16*)d_out);
}
```
